```python
import torch

def _softmax(x, axis=-1):
    return torch.softmax(x, dim=axis)

def _gelu(x):
    return torch.nn.functional.gelu(x, approximate='tanh')

def _silu(x):
    return torch.nn.functional.silu(x)

def _sigmoid(x):
    return torch.sigmoid(x)

def _layernorm(x, axis=-1, eps=1e-05):
    m = x.mean(dim=axis, keepdim=True)
    v = x.var(dim=axis, keepdim=True, unbiased=False)
    return (x - m) / torch.sqrt(v + eps)

def _scatter_add(tgt, idx, src, axis=0):
    return tgt.clone().index_add_(axis, idx, src)

def _segment_sum(src, seg, n):
    out = torch.zeros((n,) + src.shape[1:], dtype=src.dtype)
    return out.index_add_(0, seg, src)

def _pad(x, pw):
    flat = tuple((v for pair in reversed(pw) for v in pair))
    return torch.nn.functional.pad(x, flat)
kernel_name = 'concat_heads__memory_aug_recurrent_mem_tr__linear_attn_block__prompt'

def setup_inputs():

    _w = lambda a: (lambda t: t.to(torch.bfloat16) if t.is_floating_point() else t)(torch.as_tensor(a))

    def _A_setup():
        B, S, Dm = (1, 2048, 448)
        H, Dh = (16, 256)
        M, seg = (8, 32)
        seg = min(seg, S)
        S = S // seg * seg
        return {'x': _w((lambda *s: torch.randn(*s, dtype=torch.bfloat16) * 0.05)(B, S, Dm)), 'mem0': _w((lambda *s: torch.randn(*s, dtype=torch.bfloat16) * 0.8)(B, M, Dm)), 'Wq': _w((lambda *s: torch.randn(*s, dtype=torch.bfloat16) * 1.6)(Dm, H, Dh) * Dm ** (-0.5)), 'Wk': _w((lambda *s: torch.randn(*s, dtype=torch.bfloat16) * 1.6)(Dm, H, Dh) * Dm ** (-0.5)), 'Wv': _w((lambda *s: torch.randn(*s, dtype=torch.bfloat16) * 0.8)(Dm, H, Dh) * Dm ** (-0.5)), 'Wo': _w((lambda *s: torch.randn(*s, dtype=torch.bfloat16) * 0.8)(H, Dh, Dm) * (H * Dh) ** (-0.5)), 'seg_len': seg}
    _A_in = _A_setup()

    def _B_setup():
        B, S, D = (1, 2048, 448)
        return {'x': _w((lambda *s: torch.randn(*s, dtype=torch.bfloat16) * 0.05)(B, S, D)), 'wqkv': _w((lambda *s: torch.randn(*s, dtype=torch.bfloat16) * 1.6)(D, 3 * D)), 'wo': _w((lambda *s: torch.randn(*s, dtype=torch.bfloat16) * 0.1)(D, D))}
    _B_in = _B_setup()
    return {'A_x': _A_in['x'], 'A_mem0': _A_in['mem0'], 'A_Wq': _A_in['Wq'], 'A_Wk': _A_in['Wk'], 'A_Wv': _A_in['Wv'], 'A_Wo': _A_in['Wo'], 'A_seg_len': _A_in['seg_len'], 'B_x': _B_in['x'], 'B_wqkv': _B_in['wqkv'], 'B_wo': _B_in['wo']}

def reference(A_x, A_mem0, A_Wq, A_Wk, A_Wv, A_Wo, A_seg_len, B_x, B_wqkv, B_wo):

    def _A_ref(x, mem0, Wq, Wk, Wv, Wo, seg_len):
        B, S, Dm = x.shape
        H, Dh = (Wq.shape[1], Wq.shape[2])
        M = mem0.shape[1]
        scale = Dh ** (-0.5)
        n_seg = S // seg_len
        L = M + seg_len
        idx = torch.arange(L)
        mask = (idx[:, None] < M) | (idx[:, None] >= idx[None, :])
        mem = mem0
        outs = []
        for s in range(n_seg):
            xs = x[:, s * seg_len:(s + 1) * seg_len, :].clone()
            h = torch.concatenate([mem, xs], axis=1)
            q = torch.einsum('btd,dhe->bhte', h, Wq)
            k = torch.einsum('btd,dhe->bhte', h, Wk)
            v = torch.einsum('btd,dhe->bhte', h, Wv)
            scores = torch.einsum('bhqe,bhke->bhqk', q, k) * scale
            scores = torch.where(mask[None, None], scores, -1000000000.0)
            p = _softmax(scores, axis=-1)
            o = torch.einsum('bhqk,bhke->bhqe', p, v)
            o = torch.einsum('bhte,hed->btd', o, Wo)
            mem = o[:, :M, :].clone()
            outs.append(o[:, M:, :])
        return torch.concatenate(outs, axis=1)

    def _B_ref(x, wqkv, wo):
        B, S, D = x.shape
        H = 16
        Dh = D // H
        qkv = (lambda x, *d: x.permute(*d))((x @ wqkv).reshape(B, S, 3, H, Dh), 2, 0, 3, 1, 4)
        q, k, v = (qkv[0], qkv[1], qkv[2])
        phi = lambda t: torch.where(t > 0, t + 1, torch.exp(t)) + 1e-06
        pq, pk = (phi(q), phi(k))
        kv = torch.einsum('bhsd,bhse->bhde', pk, v)
        z = torch.einsum('bhsd->bhd', pk)
        num = torch.einsum('bhqd,bhde->bhqe', pq, kv)
        den = torch.einsum('bhqd,bhd->bhq', pq, z)[..., None] + 1e-06
        o = num / den
        return (lambda x, *d: x.permute(*d))(o, 0, 2, 1, 3).reshape(B, S, D) @ wo
    a = _A_ref(A_x, A_mem0, A_Wq, A_Wk, A_Wv, A_Wo, A_seg_len)
    b = _B_ref(B_x, B_wqkv, B_wo)
    return torch.concatenate([a, b], axis=-1)
```

```cpp
#include <hip/hip_runtime.h>
#include <hip/hip_bf16.h>
#include <stdint.h>

typedef unsigned short u16;
__device__ __forceinline__ float bf2f(u16 b) { return __uint_as_float(((unsigned)b) << 16); }
__device__ __forceinline__ u16 f2bf(float f) {
  unsigned u = __float_as_uint(f);
  if ((u & 0x7fffffffu) > 0x7f800000u) return (u16)((u >> 16) | 0x40);
  u += 0x7fffu + ((u >> 16) & 1u);
  return (u16)(u >> 16);
}
__device__ __forceinline__ float rbf(float f) { return bf2f(f2bf(f)); }

constexpr int D = 448, H = 16, E = 256, M = 8, L = 32, NSEG = 64, S = 2048;
constexpr int NW = H * D;

__global__ void k_prep(const u16* Wq, const u16* Wk, const u16* Wv, const u16* Wo, u16* Wc) {
  int idx = blockIdx.x * blockDim.x + threadIdx.x;
  if (idx >= 2 * NW * D) return;
  int row = idx / D, col = idx % D;
  float acc = 0.f;
  if (row < NW) {
    int h = row / D, a = row % D, b = col;
    const u16* q = Wq + ((size_t)a * H + h) * E; const u16* k = Wk + ((size_t)b * H + h) * E;
    for (int e = 0; e < E; ++e) acc += bf2f(q[e]) * bf2f(k[e]);
    acc *= 0.0625f;
  } else {
    int r = row - NW; int h = r / D, n = r % D, a = col;
    const u16* v = Wv + ((size_t)a * H + h) * E; const u16* o = Wo + (size_t)h * E * D + n;
    for (int e = 0; e < E; ++e) acc += bf2f(v[e]) * bf2f(o[(size_t)e * D]);
  }
  Wc[idx] = f2bf(acc);
}
__global__ void k_gemm(const u16* X, const u16* Wc, u16* KV, int nrows, int tbase) {
  int col = blockIdx.x * blockDim.x + threadIdx.x;
  int t = blockIdx.y;
  if (col >= 2 * NW) return;
  const u16* x = X + (size_t)t * D; const u16* w = Wc + (size_t)col * D;
  float acc = 0.f;
  for (int k = 0; k < D; ++k) acc += bf2f(x[k]) * bf2f(w[k]);
  KV[(size_t)(tbase + t) * 2 * NW + col] = f2bf(acc);
}
__global__ void __launch_bounds__(1024) k_rec(const u16* mem0, const u16* Wc, const u16* KV, u16* KVm) {
  __shared__ float mem[M][D];
  __shared__ float P[H][M][40];
  __shared__ float nm[M][D];
  const int tid = threadIdx.x;
  for (int i = tid; i < M * D; i += 1024) mem[i / D][i % D] = bf2f(mem0[i]);
  __syncthreads();
  for (int s = 0; s < NSEG; ++s) {
    for (int col = tid; col < 2 * NW; col += 1024) {
      const u16* w = Wc + (size_t)col * D;
      float acc[M];
      for (int j = 0; j < M; ++j) acc[j] = 0.f;
      for (int k = 0; k < D; k += 2) {
        unsigned ww = *(const unsigned*)(w + k);
        float w0 = __uint_as_float(ww << 16), w1 = __uint_as_float(ww & 0xffff0000u);
        for (int j = 0; j < M; ++j) acc[j] += mem[j][k] * w0 + mem[j][k + 1] * w1;
      }
      for (int j = 0; j < M; ++j) KVm[(size_t)(s * M + j) * 2 * NW + col] = f2bf(acc[j]);
    }
    __threadfence();
    __syncthreads();
    for (int idx = tid; idx < H * M * 40; idx += 1024) {
      int h = idx / (M * 40), i = (idx / 40) % M, r = idx % 40;
      const u16* kk = (r < M) ? KVm + (size_t)(s * M + r) * 2 * NW + h * D : KV + (size_t)(s * L + r - M) * 2 * NW + h * D;
      float acc = 0.f;
      for (int a = 0; a < D; ++a) acc += mem[i][a] * bf2f(kk[a]);
      P[h][i][r] = acc;
    }
    __syncthreads();
    if (tid < H * M) {
      int h = tid / M, i = tid % M;
      float m = -1e30f; for (int r = 0; r < 40; ++r) m = fmaxf(m, P[h][i][r]);
      float l = 0.f; for (int r = 0; r < 40; ++r) { float e = __expf(P[h][i][r] - m); P[h][i][r] = e; l += e; }
      float rl = 1.f / l; for (int r = 0; r < 40; ++r) P[h][i][r] = rbf(P[h][i][r] * rl);
    }
    __syncthreads();
    for (int idx = tid; idx < M * D; idx += 1024) {
      int i = idx / D, n = idx % D;
      float acc = 0.f;
      for (int h = 0; h < H; ++h) for (int r = 0; r < 40; ++r) {
        const u16* vv = (r < M) ? KVm + (size_t)(s * M + r) * 2 * NW + NW + h * D : KV + (size_t)(s * L + r - M) * 2 * NW + NW + h * D;
        acc += P[h][i][r] * bf2f(vv[n]);
      }
      nm[i][n] = rbf(acc);
    }
    __syncthreads();
    for (int i = tid; i < M * D; i += 1024) mem[i / D][i % D] = nm[i / D][i % D];
    __syncthreads();
  }
}
__global__ void __launch_bounds__(1024) k_tok(const u16* X, const u16* KV, const u16* KVm, u16* out) {
  __shared__ float P[H][L][40];
  const int s = blockIdx.x, tid = threadIdx.x;
  for (int idx = tid; idx < H * L * 40; idx += 1024) {
    int h = idx / (L * 40), t = (idx / 40) % L, r = idx % 40;
    float acc;
    if (r >= M && (r - M) > t) acc = -1e9f;
    else {
      const u16* kk = (r < M) ? KVm + (size_t)(s * M + r) * 2 * NW + h * D : KV + (size_t)(s * L + r - M) * 2 * NW + h * D;
      const u16* x = X + (size_t)(s * L + t) * D;
      acc = 0.f;
      for (int a = 0; a < D; ++a) acc += bf2f(x[a]) * bf2f(kk[a]);
    }
    P[h][t][r] = acc;
  }
  __syncthreads();
  if (tid < H * L) {
    int h = tid / L, t = tid % L;
    float m = -1e30f; for (int r = 0; r < 40; ++r) m = fmaxf(m, P[h][t][r]);
    float l = 0.f; for (int r = 0; r < 40; ++r) { float e = __expf(P[h][t][r] - m); P[h][t][r] = e; l += e; }
    float rl = 1.f / l; for (int r = 0; r < 40; ++r) P[h][t][r] = rbf(P[h][t][r] * rl);
  }
  __syncthreads();
  for (int idx = tid; idx < L * D; idx += 1024) {
    int t = idx / D, n = idx % D;
    float acc = 0.f;
    for (int h = 0; h < H; ++h) for (int r = 0; r < 40; ++r) {
      const u16* vv = (r < M) ? KVm + (size_t)(s * M + r) * 2 * NW + NW + h * D : KV + (size_t)(s * L + r - M) * 2 * NW + NW + h * D;
      acc += P[h][t][r] * bf2f(vv[n]);
    }
    out[(size_t)(s * L + t) * (2 * D) + n] = f2bf(acc);
  }
}
__global__ void k_bqkv(const u16* X, const u16* W, u16* qkv) {
  int col = blockIdx.x * blockDim.x + threadIdx.x; int t = blockIdx.y;
  if (col >= 3 * D) return;
  float acc = 0.f;
  for (int k = 0; k < D; ++k) acc += bf2f(X[(size_t)t * D + k]) * bf2f(W[(size_t)k * 3 * D + col]);
  float v = rbf(acc);
  if (col < 2 * D) { v = v > 0.f ? rbf(v + 1.f) : rbf(__expf(v)); v = rbf(v + 1e-6f); }
  qkv[(size_t)t * 3 * D + col] = f2bf(v);
}
__global__ void k_bkv(const u16* qkv, float* kv, float* z) {
  int idx = blockIdx.x * blockDim.x + threadIdx.x;
  if (idx >= 16 * 28 * 29) return;
  int h = idx / (28 * 29), d = (idx / 29) % 28, e = idx % 29;
  float acc = 0.f;
  for (int t = 0; t < S; ++t) {
    float pk = bf2f(qkv[(size_t)t * 3 * D + D + h * 28 + d]);
    float v = e < 28 ? bf2f(qkv[(size_t)t * 3 * D + 2 * D + h * 28 + e]) : 1.f;
    acc += pk * v;
  }
  if (e < 28) kv[(h * 28 + d) * 28 + e] = rbf(acc); else z[h * 28 + d] = rbf(acc);
}
__global__ void k_bo(const u16* qkv, const float* kv, const float* z, u16* o) {
  int col = blockIdx.x * blockDim.x + threadIdx.x; int t = blockIdx.y;
  if (col >= D) return;
  int h = col / 28, e = col % 28;
  float num = 0.f, den = 0.f;
  for (int d = 0; d < 28; ++d) { float pq = bf2f(qkv[(size_t)t * 3 * D + h * 28 + d]); num += pq * kv[(h * 28 + d) * 28 + e]; den += pq * z[h * 28 + d]; }
  num = rbf(num); den = rbf(rbf(den) + 1e-6f);
  o[(size_t)t * D + col] = f2bf(num / den);
}
__global__ void k_bout(const u16* o, const u16* wo, u16* out) {
  int col = blockIdx.x * blockDim.x + threadIdx.x; int t = blockIdx.y;
  if (col >= D) return;
  float acc = 0.f;
  for (int k = 0; k < D; ++k) acc += bf2f(o[(size_t)t * D + k]) * bf2f(wo[(size_t)k * D + col]);
  out[(size_t)t * 2 * D + D + col] = f2bf(acc);
}

extern "C" void kernel_launch(void* const* d_in, const int* in_sizes, int n_in, void* d_out, int out_size, void* d_ws, size_t ws_size, hipStream_t stream) {
  const u16* Ax = (const u16*)d_in[0]; const u16* mem0 = (const u16*)d_in[1];
  const u16* Wq = (const u16*)d_in[2]; const u16* Wk = (const u16*)d_in[3]; const u16* Wv = (const u16*)d_in[4]; const u16* Wo = (const u16*)d_in[5];
  const u16* Bx = (const u16*)d_in[7]; const u16* wqkv = (const u16*)d_in[8]; const u16* wo = (const u16*)d_in[9];
  u16* out = (u16*)d_out;
  unsigned char* ws = (unsigned char*)d_ws;
  u16* Wc = (u16*)ws;
  u16* KV = (u16*)(ws + (16u << 20));
  u16* KVm = (u16*)(ws + (80u << 20));
  u16* qkv = (u16*)(ws + (96u << 20));
  float* kv = (float*)(ws + (104u << 20)); float* z = kv + 16 * 28 * 28;
  u16* ob = (u16*)(ws + (105u << 20));
  k_prep<<<(2 * NW * D + 255) / 256, 256, 0, stream>>>(Wq, Wk, Wv, Wo, Wc);
  k_gemm<<<dim3(2 * NW / 256, S), 256, 0, stream>>>(Ax, Wc, KV, S, 0);
  k_rec<<<1, 1024, 0, stream>>>(mem0, Wc, KV, KVm);
  k_tok<<<NSEG, 1024, 0, stream>>>(Ax, KV, KVm, out);
  k_bqkv<<<dim3((3 * D + 255) / 256, S), 256, 0, stream>>>(Bx, wqkv, qkv);
  k_bkv<<<(16 * 28 * 29 + 255) / 256, 256, 0, stream>>>(qkv, kv, z);
  k_bo<<<dim3(2, S), 256, 0, stream>>>(qkv, kv, z, ob);
  k_bout<<<dim3(2, S), 256, 0, stream>>>(ob, wo, out);
}
```

```cpp
#include <hip/hip_runtime.h>
#include <hip/hip_bf16.h>
#include <stdint.h>

typedef unsigned short u16;
typedef unsigned v4u __attribute__((ext_vector_type(4)));
typedef unsigned v2u __attribute__((ext_vector_type(2)));
typedef float f32x4 __attribute__((ext_vector_type(4)));
typedef float f32x2 __attribute__((ext_vector_type(2)));
typedef short bf16x8 __attribute__((ext_vector_type(8)));
typedef short s16x4 __attribute__((ext_vector_type(4)));
typedef __bf16 bf16x2_t __attribute__((ext_vector_type(2)));
#define GAS __attribute__((address_space(1)))
#define LAS __attribute__((address_space(3)))
#define DI __device__ __forceinline__
#define RLX_AGENT __ATOMIC_RELAXED, __HIP_MEMORY_SCOPE_AGENT

constexpr int D = 448, H = 16, E = 256, NSEG = 64, SEQ = 2048;
constexpr unsigned SENT = 0xFFFFFFFFu;
constexpr int AUX_SC1 = 16;
constexpr float LOG2E = 1.4426950408889634f;
constexpr unsigned SPIN_CAP = 1u << 20;

constexpr size_t XCH_OFF = 0;
constexpr size_t CTRL_OFF = 1u << 20;
constexpr size_t WF_OFF = 2u << 20;
constexpr size_t KKF_OFF = 16u << 20;
constexpr size_t VVF_OFF = 46u << 20;
constexpr size_t KKM_OFF = 76u << 20;
constexpr size_t VMF_OFF = 84u << 20;
constexpr size_t SXX_OFF = 92u << 20;
constexpr size_t KVP_OFF = 97u << 20;
constexpr size_t WOF_OFF = 98u << 20;
constexpr size_t PQ_OFF = 100u << 20;
constexpr size_t DBG_OFF = 120u << 20;
constexpr size_t WS_USED = 128u << 20;
constexpr int CW_CLASS = 0;
constexpr int CW_TMO = 32;
constexpr int CW_PROG = 64;
constexpr int CW_PM = CW_PROG + 64 * 32;
constexpr int CW_B1 = CW_PM + 96 * 32;
constexpr int CW_END = CW_B1 + 64;

DI unsigned pk2(float a, float b) { f32x2 v = {a, b}; bf16x2_t r = __builtin_convertvector(v, bf16x2_t); return __builtin_bit_cast(unsigned, r); }
DI f32x4 mfma(v4u a, v4u b, f32x4 c) { return __builtin_amdgcn_mfma_f32_16x16x32_bf16(__builtin_bit_cast(bf16x8, a), __builtin_bit_cast(bf16x8, b), c, 0, 0, 0); }
DI v4u ld128s(__amdgpu_buffer_rsrc_t rs, int off) { return __builtin_amdgcn_raw_buffer_load_b128(rs, off, 0, AUX_SC1); }
DI v2u ld64s(__amdgpu_buffer_rsrc_t rs, int off) { return __builtin_amdgcn_raw_buffer_load_b64(rs, off, 0, AUX_SC1); }
DI void st128s(v4u v, __amdgpu_buffer_rsrc_t rs, int off) { __builtin_amdgcn_raw_buffer_store_b128(v, rs, off, 0, AUX_SC1); }
DI void st64s(v2u v, __amdgpu_buffer_rsrc_t rs, int off) { __builtin_amdgcn_raw_buffer_store_b64(v, rs, off, 0, AUX_SC1); }
DI void st32s(unsigned v, __amdgpu_buffer_rsrc_t rs, int off) { __builtin_amdgcn_raw_buffer_store_b32(v, rs, off, 0, AUX_SC1); }
DI unsigned canon(unsigned x) { return x < 0xFFFFFFFEu ? x : 0xFFFFFFFEu; }
DI unsigned umax(unsigned a, unsigned b) { return a > b ? a : b; }
DI unsigned mx4(v4u v) { return umax(umax(v.x, v.y), umax(v.z, v.w)); }
DI bool ok4(v4u v) { return mx4(v) != SENT; }
DI void lbar() { asm volatile("s_waitcnt lgkmcnt(0)\n\ts_barrier" ::: "memory"); }
DI void vdrain() { asm volatile("s_waitcnt vmcnt(0)" ::: "memory"); }
#define GLD3(e, sm, m, off16, off4, base) asm volatile("global_load_dwordx4 %0, %3, %5 offset:256 sc1\n\tglobal_load_dwordx4 %1, %3, %5 sc1\n\tglobal_load_dword %2, %4, %5 offset:384 sc1" : "+v"(e), "+v"(sm), "+v"(m) : "v"(off16), "v"(off4), "s"(base) : "memory")
#define GWAIT3(n, e, sm, m) asm volatile("s_waitcnt vmcnt(" #n ")" : "+v"(e), "+v"(sm), "+v"(m) :: "memory")
#define GLD2(a, b, off, base) asm volatile("global_load_dwordx4 %0, %2, %3 sc1\n\tglobal_load_dwordx4 %1, %2, %3 offset:1024 sc1" : "+v"(a), "+v"(b) : "v"(off), "s"(base) : "memory")
#define GWAIT2(n, a, b) asm volatile("s_waitcnt vmcnt(" #n ")" : "+v"(a), "+v"(b) :: "memory")
DI float swap32_add(float x) { auto r = __builtin_amdgcn_permlane32_swap(__float_as_uint(x), __float_as_uint(x), false, false); return __uint_as_float(r[0]) + __uint_as_float(r[1]); }
DI float swap16_add(float x) { auto r = __builtin_amdgcn_permlane16_swap(__float_as_uint(x), __float_as_uint(x), false, false); return __uint_as_float(r[0]) + __uint_as_float(r[1]); }
DI float swap32_max(float x) { auto r = __builtin_amdgcn_permlane32_swap(__float_as_uint(x), __float_as_uint(x), false, false); return fmaxf(__uint_as_float(r[0]), __uint_as_float(r[1])); }
DI float swap16_max(float x) { auto r = __builtin_amdgcn_permlane16_swap(__float_as_uint(x), __float_as_uint(x), false, false); return fmaxf(__uint_as_float(r[0]), __uint_as_float(r[1])); }
DI float ror8(float x) { return __uint_as_float((unsigned)__builtin_amdgcn_update_dpp(0, (int)__float_as_uint(x), 0x128, 0xf, 0xf, false)); }
DI float ex2(float x) { return __builtin_amdgcn_exp2f(x); }
DI int xch_off(int slot, int blk, int cls) { return ((slot * 5 + blk) * 2 + cls) * 8192; }

struct Params {
  const u16* x; const u16* mem0; const u16* Wq; const u16* Wk; const u16* Wv; const u16* Wo;
  const u16* bx; const u16* wqkv; const u16* wo;
  u16* out; unsigned char* ws;
};

template <bool ISA>
DI void prep_block(const Params& p, unsigned char* lds, int m, int ra, int cb) {
  const int tid = threadIdx.x, lane = tid & 63, w = __builtin_amdgcn_readfirstlane(tid >> 6);
  const int h = m & 15, r16 = lane & 15, gq = lane >> 4;
  const u16* rowsrc = ISA ? p.Wk : p.Wv;
  v4u ra0[8], ra1[8];
  if (w < 7) {
    const int row0 = 32 * (7 * cb + w) + r16;
#pragma unroll
    for (int es = 0; es < 8; ++es) {
      ra0[es] = *(const v4u*)(rowsrc + ((size_t)row0 * H + h) * E + 32 * es + 8 * gq);
      ra1[es] = *(const v4u*)(rowsrc + ((size_t)(row0 + 16) * H + h) * E + 32 * es + 8 * gq);
    }
  }
  if constexpr (ISA) {
#pragma unroll
    for (int i = 0; i < 7; ++i) {
      const int c = tid + 512 * i, fr = c >> 6, l = c & 63, tau = fr >> 3, es = fr & 7;
      const int a = 112 * ra + 16 * tau + (l & 15);
      v4u v = *(const v4u*)(p.Wq + ((size_t)a * H + h) * E + 32 * es + 8 * (l >> 4));
      *(v4u*)(lds + c * 16) = v;
    }
  } else {
#pragma unroll
    for (int i = 0; i < 7; ++i) {
      const int c = tid + 512 * i, e = c / 14, ch = c - e * 14;
      v4u v = *(const v4u*)(p.Wo + ((size_t)h * E + e) * D + 112 * ra + ch * 8);
      *(v4u*)(lds + e * 224 + ch * 16) = v;
    }
  }
  __syncthreads();
  if (w < 7) {
    f32x4 acc[7][2];
#pragma unroll
    for (int t = 0; t < 7; ++t) { acc[t][0] = f32x4{0, 0, 0, 0}; acc[t][1] = f32x4{0, 0, 0, 0}; }
#pragma unroll
    for (int es = 0; es < 8; ++es) {
#pragma unroll
      for (int t = 0; t < 7; ++t) {
        v4u b;
        if constexpr (ISA) b = *(const v4u*)(lds + ((t * 8 + es) * 64 + lane) * 16);
        else {
          const int q = (lane & 15) >> 2, pp = lane & 3;
          const int base = (32 * es + 8 * gq + q) * 224 + (16 * t + 4 * pp) * 2;
          typedef short v4s __attribute__((ext_vector_type(4)));
          v4s lo = __builtin_amdgcn_ds_read_tr16_b64_v4i16((LAS v4s*)((LAS unsigned char*)lds + base));
          v4s hi = __builtin_amdgcn_ds_read_tr16_b64_v4i16((LAS v4s*)((LAS unsigned char*)lds + base + 4 * 224));
          v2u l2 = __builtin_bit_cast(v2u, lo), h2 = __builtin_bit_cast(v2u, hi);
          b = v4u{l2.x, l2.y, h2.x, h2.y};
        }
        acc[t][0] = mfma(ra0[es], b, acc[t][0]);
        acc[t][1] = mfma(ra1[es], b, acc[t][1]);
      }
    }
    const float sc = ISA ? 0.0625f : 1.0f;
    const int gq2 = (gq >> 1) + 2 * (gq & 1);
    unsigned char* wf = p.ws + WF_OFF;
#pragma unroll
    for (int t = 0; t < 7; ++t) {
      unsigned x0 = pk2(acc[t][0][0] * sc, acc[t][0][1] * sc), x1 = pk2(acc[t][0][2] * sc, acc[t][0][3] * sc);
      unsigned y0 = pk2(acc[t][1][0] * sc, acc[t][1][1] * sc), y1 = pk2(acc[t][1][2] * sc, acc[t][1][3] * sc);
      auto s0 = __builtin_amdgcn_permlane16_swap(x0, y0, false, false);
      auto s1 = __builtin_amdgcn_permlane16_swap(x1, y1, false, false);
      v4u o = {s0[0], s1[0], s0[1], s1[1]};
      *(v4u*)(wf + ((size_t)((m * 28 + 7 * ra + t) * 14 + 7 * cb + w) * 64 + r16 + 16 * gq2) * 16) = o;
    }
  }
}

__global__ void __launch_bounds__(512, 2) k_prep(Params p) {
  extern __shared__ __attribute__((aligned(16))) unsigned char lds[];
  const int b = blockIdx.x, tid = threadIdx.x;
  if (b < 256) {
    const int label = b & 7, rank = b >> 3, m = label * 4 + (rank >> 3), blk = rank & 7;
    if (m < 16) prep_block<true>(p, lds, m, blk >> 1, blk & 1);
    else prep_block<false>(p, lds, m, blk >> 1, blk & 1);
    return;
  }
  if (b == 256) {
    unsigned* cw = (unsigned*)(p.ws + CTRL_OFF);
    for (int i = tid; i < CW_END; i += 512) if (i > 2) cw[i] = 0;
    v4u s = {SENT, SENT, SENT, SENT};
    for (int i = tid; i < 40 * 512; i += 512) {
      const int blk = i >> 9, c = i & 511;
      v4u v = s;
      if (blk < 2 && c < 448) {
        const int ksq = c >> 6, l = c & 63, j = l & 7, hf = (l >> 3) & 1, gq = l >> 4;
        v = *(const v4u*)(p.mem0 + j * D + 32 * (ksq + 7 * hf) + 8 * gq);
        v.x = canon(v.x); v.y = canon(v.y); v.z = canon(v.z); v.w = canon(v.w);
      }
      *(v4u*)(p.ws + XCH_OFF + (size_t)blk * 8192 + c * 16) = v;
    }
    return;
  }
  if (b == 257) {
    if (tid < 64) {
      unsigned best[2] = {1u << 30, 1u << 30};
      for (int rep = 0; rep < 12; ++rep) {
        const int cls = rep & 1;
        unsigned* q = (unsigned*)(p.ws + XCH_OFF + (size_t)(40 + 2 * (rep >> 1 & 3) + cls) * 8192);
        __hip_atomic_store((GAS unsigned*)q, (unsigned)rep, RLX_AGENT);
        vdrain();
        unsigned long long t0 = __builtin_amdgcn_s_memtime();
        unsigned x = __hip_atomic_load((GAS unsigned*)q, RLX_AGENT);
        asm volatile("s_waitcnt vmcnt(0)" :: "v"(x) : "memory");
        unsigned long long t1 = __builtin_amdgcn_s_memtime();
        unsigned d = (unsigned)(t1 - t0);
        best[cls] = d < best[cls] ? d : best[cls];
      }
      if (tid == 0) { ((unsigned*)(p.ws + CTRL_OFF))[CW_CLASS] = best[1] < best[0] ? 1u : 0u;
        ((unsigned*)(p.ws + CTRL_OFF))[1] = best[0]; ((unsigned*)(p.ws + CTRL_OFF))[2] = best[1]; }
    }
    return;
  }
}

struct Ctx { __amdgpu_buffer_rsrc_t rs; GAS unsigned* cw; int cls; };

DI bool wait_ge(GAS unsigned* word, unsigned want, GAS unsigned* tmo) {
  for (unsigned spins = 0;; ++spins) {
    unsigned v = __builtin_amdgcn_readfirstlane(__hip_atomic_load(word, RLX_AGENT));
    if (v >= want) return true;
    if (spins > SPIN_CAP) { __hip_atomic_store(tmo, 1u, RLX_AGENT); return false; }
    if ((spins & 1023u) == 1023u && __builtin_amdgcn_readfirstlane(__hip_atomic_load(tmo, RLX_AGENT))) return false;
    __builtin_amdgcn_s_sleep(2);
  }
}
DI bool poll128(__amdgpu_buffer_rsrc_t rs, int off, v4u& m, GAS unsigned* tmo) {
  unsigned spins = 0;
  bool good = true;
  do {
    asm volatile("" ::: "memory");
    m = ld128s(rs, off);
    if (++spins > SPIN_CAP) { good = false; break; }
  } while (!ok4(m));
  if (!__all(good)) { __hip_atomic_store(tmo, 1u, RLX_AGENT); return false; }
  return true;
}

constexpr int L_MEM = 0;
constexpr int L_C = 8192;
constexpr int L_MISC = 8192 + 4096;

constexpr int L_P = 8192;
constexpr int L_F = 8192 + 2048;
DI void role_S(const Params& p, const Ctx& c, unsigned char* lds, int h, int g) {
  const int tid = threadIdx.x, lane = tid & 63, w = __builtin_amdgcn_readfirstlane(tid >> 6);
  const int gq = lane >> 4, c16 = lane & 15;
  const unsigned char* wf = p.ws + WF_OFF;
  GAS unsigned* tmo = c.cw + CW_TMO;
  const int rec = (h * 4 + g) * 512;
  const int recblk = 1 + (rec >> 13), recin = rec & 8191;
  bool alive = true;
  if (tid < 4) *(unsigned*)(lds + L_F + tid * 4) = 0;
  if (w < 4) {
    v4u wa[2][14];
#pragma unroll
    for (int t2 = 0; t2 < 2; ++t2)
#pragma unroll
      for (int ks = 0; ks < 14; ++ks) {
        const int tile = (w == 3 && t2 == 1) ? 6 : 2 * w + t2;
        wa[t2][ks] = *(const v4u*)(wf + ((size_t)((h * 28 + 7 * g + tile) * 14 + ks) * 64 + lane) * 16);
      }
    int maddr[2];
#pragma unroll
    for (int t2 = 0; t2 < 2; ++t2) {
      const int tile = (w == 3 && t2 == 1) ? 6 : 2 * w + t2;
      const int a = 112 * g + 16 * tile + 4 * gq, ks = a >> 5, ksq = ks >= 7 ? ks - 7 : ks, hf = ks >= 7 ? 1 : 0;
      maddr[t2] = L_MEM + ksq * 1024 + (((a & 31) >> 3) * 16 + hf * 8 + (c16 & 7)) * 16 + (a & 7) * 2;
    }
    for (int s = 0; s < NSEG; ++s) {
      const int slot = s & 3;
      vdrain();
      lbar();
      v4u mf[7];
#pragma unroll
      for (int k = 0; k < 7; ++k) mf[k] = *(const v4u*)(lds + L_MEM + k * 1024 + lane * 16);
      const v2u mb0 = *(const v2u*)(lds + maddr[0]), mb1 = *(const v2u*)(lds + maddr[1]);
      float v0[4], v1[4];
      f32x4 d2;
      if (w < 3) {
        f32x4 a1 = {0, 0, 0, 0}, a2 = {0, 0, 0, 0}, b1 = {0, 0, 0, 0}, b2 = {0, 0, 0, 0};
#pragma unroll
        for (int k = 0; k < 7; ++k) {
          a1 = mfma(wa[0][k], mf[k], a1); a2 = mfma(wa[0][k + 7], mf[k], a2);
          b1 = mfma(wa[1][k], mf[k], b1); b2 = mfma(wa[1][k + 7], mf[k], b2);
        }
#pragma unroll
        for (int r = 0; r < 4; ++r) { v0[r] = (lane & 8) ? a2[r] : a1[r]; v1[r] = (lane & 8) ? b2[r] : b1[r]; }
        d2 = mfma(v4u{pk2(v0[0], v0[1]), pk2(v0[2], v0[3]), pk2(v1[0], v1[1]), pk2(v1[2], v1[3])}, v4u{mb0.x, mb0.y, mb1.x, mb1.y}, f32x4{0, 0, 0, 0});
      } else {
        f32x4 a1 = {0, 0, 0, 0}, a2 = {0, 0, 0, 0};
#pragma unroll
        for (int k = 0; k < 7; ++k) { a1 = mfma(wa[0][k], mf[k], a1); a2 = mfma(wa[0][k + 7], mf[k], a2); }
#pragma unroll
        for (int r = 0; r < 4; ++r) { v0[r] = (lane & 8) ? a2[r] : a1[r]; v1[r] = 0.f; }
        d2 = mfma(v4u{pk2(v0[0], v0[1]), pk2(v0[2], v0[3]), 0u, 0u}, v4u{mb0.x, mb0.y, 0u, 0u}, f32x4{0, 0, 0, 0});
      }
      {
        f32x4 o;
#pragma unroll
        for (int r = 0; r < 4; ++r) o[r] = swap32_add(d2[r]);
        if (c16 < 8 && gq < 2) *(f32x4*)(lds + L_P + ((w * 2 + gq) * 8 + c16) * 16) = o;
      }
      lbar();
      {
        const int cb = (s * 16 + h) * 56 + 14 * g + 4 * w + (gq >> 1);
        float f[4];
#pragma unroll
        for (int r = 0; r < 4; ++r) f[r] = v0[r] + ror8(v0[r]);
        if (c16 < 8) st64s(v2u{pk2(f[0], f[1]), pk2(f[2], f[3])}, c.rs, (int)(KKM_OFF + (size_t)cb * 128 + c16 * 16 + (gq & 1) * 8));
        if (w < 3) {
#pragma unroll
          for (int r = 0; r < 4; ++r) f[r] = v1[r] + ror8(v1[r]);
          if (c16 < 8) st64s(v2u{pk2(f[0], f[1]), pk2(f[2], f[3])}, c.rs, (int)(KKM_OFF + (size_t)(cb + 2) * 128 + c16 * 16 + (gq & 1) * 8));
        }
      }
      if (!alive) break;
    }
    vdrain();
    lbar();
  } else {
    const int u = w - 4;
    unsigned kkprog = 0;
    v4u kf[7];
    auto load_kk = [&](int s) -> bool {
      if (kkprog < (unsigned)s + 1) {
        if (!wait_ge(c.cw + CW_PROG + (2 * h) * 32, s + 1, tmo)) return false;
        if (!wait_ge(c.cw + CW_PROG + (2 * h + 1) * 32, s + 1, tmo)) return false;
        kkprog = s + 1;
      }
#pragma unroll
      for (int k = 0; k < 7; ++k) kf[k] = ld128s(c.rs, (int)(KKF_OFF + ((size_t)(((s * 16 + h) * 4 + g) * 7 + k) * 64 + lane) * 16));
      return true;
    };
    if (w == 7) alive = load_kk(0);
    for (int s = 0; s < NSEG; ++s) {
      const int slot = s & 3;
      {
        const int o0 = xch_off(slot, 0, c.cls) + (2 * u) * 1024 + lane * 16;
        v4u m0, m1;
        unsigned spins = 0; bool good;
        do {
          asm volatile("" ::: "memory");
          m0 = ld128s(c.rs, o0);
          if (u < 3) m1 = ld128s(c.rs, o0 + 1024); else m1 = m0;
          good = umax(mx4(m0), mx4(m1)) != SENT;
          if (++spins > SPIN_CAP) { __hip_atomic_store(tmo, 1u, RLX_AGENT); alive = false; break; }
        } while (!good);
        *(v4u*)(lds + L_MEM + (2 * u) * 1024 + lane * 16) = m0;
        if (u < 3) *(v4u*)(lds + L_MEM + (2 * u + 1) * 1024 + lane * 16) = m1;
        alive = __all(alive);
      }
      lbar();
      if (w == 5 && s > 0 && lane == 0) __hip_atomic_store(c.cw + CW_PM + (h * 4 + g) * 32, (unsigned)s, RLX_AGENT);
      if (w == 7 && s < NSEG - 1) {
        __builtin_amdgcn_s_setprio(2);
        v4u mf[7];
#pragma unroll
        for (int k = 0; k < 7; ++k) mf[k] = *(const v4u*)(lds + L_MEM + k * 1024 + lane * 16);
        f32x4 ax = {0, 0, 0, 0};
#pragma unroll
        for (int k = 0; k < 7; ++k) ax = mfma(kf[k], mf[k], ax);
        const bool val = (gq >> 1) == ((lane >> 3) & 1);
        float sx[4];
#pragma unroll
        for (int r = 0; r < 4; ++r) { float uu = val ? ax[r] : 0.f; uu += ror8(uu); sx[r] = swap32_add(uu); }
        float m = fmaxf(fmaxf(sx[0], sx[1]), fmaxf(sx[2], sx[3]));
        m = swap16_max(m);
        const int ro = xch_off(slot, recblk, c.cls) + recin;
        if (c16 < 8 && gq == 0) st32s(canon(__float_as_uint(m)), c.rs, ro + 384 + c16 * 4);
        const float mb = -m * LOG2E;
        float e[4];
#pragma unroll
        for (int r = 0; r < 4; ++r) e[r] = ex2(fmaf(sx[r], LOG2E, mb));
        if (c16 < 8 && gq < 2) st64s(v2u{canon(pk2(e[0], e[1])), canon(pk2(e[2], e[3]))}, c.rs, ro + 256 + c16 * 16 + gq * 8);
        __builtin_amdgcn_s_setprio(0);
      }
      lbar();
      if (w == 7 && s + 1 < NSEG - 1) alive = alive && load_kk(s + 1);
      if (w == 4 && s < NSEG - 1) {
        const int i = lane & 7, gg = (lane >> 3) & 1;
        f32x4 t0 = *(const f32x4*)(lds + L_P + ((0 + gg) * 8 + i) * 16), t1 = *(const f32x4*)(lds + L_P + ((2 + gg) * 8 + i) * 16);
        f32x4 t2 = *(const f32x4*)(lds + L_P + ((4 + gg) * 8 + i) * 16), t3 = *(const f32x4*)(lds + L_P + ((6 + gg) * 8 + i) * 16);
        f32x4 o = (t0 + t1) + (t2 + t3);
        const int ro = xch_off(slot, recblk, c.cls) + recin;
        if (lane < 16) st64s(v2u{canon(pk2(o[0], o[1])), canon(pk2(o[2], o[3]))}, c.rs, ro + i * 16 + gg * 8);
        if (lane < 26) st128s(v4u{SENT, SENT, SENT, SENT}, c.rs, xch_off((s + 2) & 3, recblk, c.cls) + recin + lane * 16);
      }
      if (!alive) break;
    }
    lbar();
    if (w == 5 && alive && lane == 0) __hip_atomic_store(c.cw + CW_PM + (h * 4 + g) * 32, (unsigned)NSEG, RLX_AGENT);
  }
}

DI void role_O(const Params& p, const Ctx& c, unsigned char* lds, int oc) {
  const int tid = threadIdx.x, lane = tid & 63, w = __builtin_amdgcn_readfirstlane(tid >> 6);
  const int gq = lane >> 4, c16 = lane & 15;
  const int hw = w;
  const unsigned char* wf = p.ws + WF_OFF;
  v4u wb[2][14];
#pragma unroll
  for (int hh = 0; hh < 2; ++hh)
#pragma unroll
    for (int ks = 0; ks < 14; ++ks) wb[hh][ks] = *(const v4u*)(wf + ((size_t)((448 + (2 * hw + hh) * 28 + oc) * 14 + ks) * 64 + lane) * 16);
  GAS unsigned* tmo = c.cw + CW_TMO;
  const int half = oc >= 14 ? 1 : 0;
  unsigned vprog = 0;
  v4u vx[2];
  auto load_vv = [&](int s) -> bool {
    if (vprog < (unsigned)s + 1) {
      if (!wait_ge(c.cw + CW_PROG + (32 + 2 * (2 * hw) + half) * 32, s + 1, tmo)) return false;
      if (!wait_ge(c.cw + CW_PROG + (32 + 2 * (2 * hw + 1) + half) * 32, s + 1, tmo)) return false;
      vprog = s + 1;
    }
#pragma unroll
    for (int hh = 0; hh < 2; ++hh) vx[hh] = ld128s(c.rs, (int)(VVF_OFF + ((size_t)((s * 28 + oc) * 16 + 2 * hw + hh) * 64 + lane) * 16));
    return true;
  };
  bool alive = load_vv(0);
  const int rh = 2 * hw + ((lane >> 3) & 1), ri = lane & 7;
  const int rec = (rh * 4 + gq) * 512, recblk = 1 + (rec >> 13), recin = rec & 8191;
  const int oks = oc >> 1, oksq = oks >= 7 ? oks - 7 : oks, ohf = oks >= 7 ? 1 : 0;
  for (int s = 0; s < NSEG; ++s) {
    const int slot = s & 3;
    if (w < 7) {
      v4u m;
      alive = alive && poll128(c.rs, xch_off(slot, 0, c.cls) + w * 1024 + lane * 16, m, tmo);
      *(v4u*)(lds + L_MEM + w * 1024 + lane * 16) = m;
    } else vdrain();
    lbar();
    if (w == 7 && s > 0 && lane == 0) __hip_atomic_store(c.cw + CW_PM + (64 + oc) * 32, (unsigned)s, RLX_AGENT);
    v4u mf[7];
#pragma unroll
    for (int k = 0; k < 7; ++k) mf[k] = *(const v4u*)(lds + L_MEM + k * 1024 + lane * 16);
    unsigned zb[2];
    v2u zz;
    {
      float z[2][4];
#pragma unroll
      for (int hh = 0; hh < 2; ++hh) {
        f32x4 aA = {0, 0, 0, 0}, aB = {0, 0, 0, 0};
#pragma unroll
        for (int k = 0; k < 7; ++k) { aA = mfma(mf[k], wb[hh][k], aA); aB = mfma(mf[k], wb[hh][k + 7], aB); }
#pragma unroll
        for (int r = 0; r < 4; ++r) z[hh][r] = swap32_add(gq < 2 ? aA[r] : aB[r]);
      }
      const unsigned p00 = pk2(z[0][0], z[0][1]), p01 = pk2(z[0][2], z[0][3]), p10 = pk2(z[1][0], z[1][1]), p11 = pk2(z[1][2], z[1][3]);
      zb[0] = gq < 2 ? p00 : p01; zb[1] = gq < 2 ? p10 : p11;
      zz = gq < 2 ? v2u{p00, p01} : v2u{p10, p11};
    }
    asm volatile("" :: "v"(zb[0]), "v"(zb[1]), "v"(zz));
    v4u pa_e = {0, 0, 0, 0}, pa_s = {0, 0, 0, 0}, pb_e = {0, 0, 0, 0}, pb_s = {0, 0, 0, 0}; unsigned pa_m = 0, pb_m = 0;
    if (s < NSEG - 1) {
      const int ro = xch_off(slot, recblk, c.cls) + recin;
      v4u c0, c1, c2; unsigned c3;
      {
        bool dA = false, dB = false;
        for (unsigned spins = 0;; ++spins) {
          asm volatile("" ::: "memory");
          if (!dA) { c3 = __builtin_amdgcn_raw_buffer_load_b32(c.rs, ro + 384 + ri * 4, 0, AUX_SC1); c2 = ld128s(c.rs, ro + 256 + ri * 16); }
          if (!dB) { c1 = ld128s(c.rs, ro + ri * 16); }
          dA = dA || __all(umax(mx4(c2), c3) != SENT);
          dB = dB || __all(mx4(c1) != SENT);
          if (dA && dB) break;
          if (spins > SPIN_CAP) { __hip_atomic_store(tmo, 1u, RLX_AGENT); alive = false; break; }
        }
      }
      float ef[8];
#pragma unroll
      for (int k = 0; k < 4; ++k) { ef[2 * k] = __uint_as_float(c2[k] << 16); ef[2 * k + 1] = __uint_as_float(c2[k] & 0xffff0000u); }
      const float mg = __uint_as_float(c3);
      const float lg = ((ef[0] + ef[1]) + (ef[2] + ef[3])) + ((ef[4] + ef[5]) + (ef[6] + ef[7]));
      const float mx = swap32_max(swap16_max(mg));
      { v4u t = c1;
        c0 = v4u{t.x << 16, t.x & 0xffff0000u, t.y << 16, t.y & 0xffff0000u};
        c1 = v4u{t.z << 16, t.z & 0xffff0000u, t.w << 16, t.w & 0xffff0000u}; }
      float x[4];
#pragma unroll
      for (int r = 0; r < 4; ++r) {
        auto q = __builtin_amdgcn_permlane16_swap(c0[r], c1[r], false, false);
        x[r] = __uint_as_float(q[0]) + __uint_as_float(q[1]);
      }
      float s0, s1;
      { auto q = __builtin_amdgcn_permlane32_swap(__float_as_uint(x[0]), __float_as_uint(x[2]), false, false); s0 = __uint_as_float(q[0]) + __uint_as_float(q[1]); }
      { auto q = __builtin_amdgcn_permlane32_swap(__float_as_uint(x[1]), __float_as_uint(x[3]), false, false); s1 = __uint_as_float(q[0]) + __uint_as_float(q[1]); }
      float m = swap32_max(swap16_max(fmaxf(s0, s1)));
      m = fmaxf(m, mx);
      const float mb = -m * LOG2E;
      const float e0 = ex2(fmaf(s0, LOG2E, mb)), e1 = ex2(fmaf(s1, LOG2E, mb)), f = ex2(fmaf(mg, LOG2E, mb));
      float l = fmaf(f, lg, e0 + e1);
      l = swap32_add(swap16_add(l));
      const float rl = __builtin_amdgcn_rcpf(l);
      const float fr = f * rl;
      v4u ax, am;
#pragma unroll
      for (int k = 0; k < 4; ++k) ax[k] = pk2(ef[2 * k] * fr, ef[2 * k + 1] * fr);
      am = v4u{pk2(e0 * rl, e1 * rl), 0u, 0u, 0u};
      f32x4 oa = {0, 0, 0, 0}, ob = {0, 0, 0, 0}, oc2 = {0, 0, 0, 0}, od = {0, 0, 0, 0};
      oa = mfma(ax, vx[0], oa); ob = mfma(ax, vx[1], ob);
      oc2 = mfma(am, v4u{zb[0], 0u, 0u, 0u}, oc2); od = mfma(am, v4u{zb[1], 0u, 0u, 0u}, od);
      float o[4];
#pragma unroll
      for (int r = 0; r < 4; ++r) o[r] = swap32_add(gq < 2 ? oa[r] + oc2[r] : ob[r] + od[r]);
      if (gq < 2) {
#pragma unroll
        for (int r = 0; r < 4; ++r) *(float*)(lds + L_C + ((w * 8 + 4 * gq + r) * 16 + c16) * 4) = o[r];
      }
      lbar();
      if (w == 7) {
        const int i = (lane >> 2) & 7, mp = (lane >> 5) * 4 + (lane & 3);
        f32x2 t[8];
#pragma unroll
        for (int k = 0; k < 8; ++k) t[k] = *(const f32x2*)(lds + L_C + ((k * 8 + i) * 16 + 2 * mp) * 4);
        f32x2 sum = ((t[0] + t[1]) + (t[2] + t[3])) + ((t[4] + t[5]) + (t[6] + t[7]));
        const int pos = oksq * 1024 + ((2 * (oc & 1) + (lane >> 5)) * 16 + ohf * 8 + i) * 16 + (lane & 3) * 4;
        st32s(canon(pk2(sum[0], sum[1])), c.rs, xch_off((s + 1) & 3, 0, c.cls) + pos);
        st32s(SENT, c.rs, xch_off((s + 3) & 3, 0, c.cls) + pos);
      }
    }
    st64s(zz, c.rs, (int)(VMF_OFF + ((size_t)((s * 28 + oc) * 8 + hw) * 64 + lane) * 8));
    if (s + 1 < NSEG - 1) alive = alive && load_vv(s + 1);
    if (!alive) break;
  }
  vdrain();
  lbar();
  if (w == 7 && alive && lane == 0) __hip_atomic_store(c.cw + CW_PM + (64 + oc) * 32, (unsigned)NSEG, RLX_AGENT);
}

DI void role_KV(const Params& p, const Ctx& c, unsigned char* lds, int hm2) {
  const int tid = threadIdx.x, lane = tid & 63, w = __builtin_amdgcn_readfirstlane(tid >> 6);
  const int gq = lane >> 4, c16 = lane & 15;
  const int m = hm2 >> 1, half = hm2 & 1;
  const bool iskk = m < 16;
  const int h = m & 15;
  const unsigned char* wf = p.ws + WF_OFF;
  constexpr int XB = 28 * 1024, L_SP = 2 * XB;
  if (w == 7) {
    auto reduce = [&](int s) {
      const unsigned char* sp = lds + L_SP + (s & 1) * XB + lane * 16;
#pragma unroll
      for (int qt = 0; qt < 2; ++qt) {
        f32x4 a0 = *(const f32x4*)(sp + qt * 1024), a1 = *(const f32x4*)(sp + (2 + qt) * 1024);
#pragma unroll
        for (int w2 = 1; w2 < 7; ++w2) { a0 += *(const f32x4*)(sp + (w2 * 4 + qt) * 1024); a1 += *(const f32x4*)(sp + (w2 * 4 + 2 + qt) * 1024); }
        st128s(v4u{pk2(a0[0], a0[1]), pk2(a0[2], a0[3]), pk2(a1[0], a1[1]), pk2(a1[2], a1[3])}, c.rs, (int)(SXX_OFF + ((size_t)(((s * 16 + h) * 2 + half) * 2 + qt) * 64 + lane) * 16));
      }
    };
    const u16* xs = p.x + (size_t)c16 * D + 8 * gq;
    v4u t[28];
#pragma unroll
    for (int f = 0; f < 28; ++f) { const int rt = f / 14, ks = f - rt * 14; t[f] = *(const v4u*)(xs + (size_t)(16 * rt) * D + 32 * ks); }
#pragma unroll
    for (int f = 0; f < 28; ++f) *(v4u*)(lds + f * 1024 + lane * 16) = t[f];
    for (int s = 0; s < NSEG; ++s) {
      lbar();
      const int sn = s + 1 < NSEG ? s + 1 : s;
#pragma unroll
      for (int f = 0; f < 28; ++f) { const int rt = f / 14, ks = f - rt * 14; t[f] = *(const v4u*)(xs + (size_t)(32 * sn + 16 * rt) * D + 32 * ks); }
      if (iskk && s > 0) reduce(s - 1);
#pragma unroll
      for (int f = 0; f < 28; ++f) *(v4u*)(lds + ((s + 1) & 1) * XB + f * 1024 + lane * 16) = t[f];
      vdrain();
      lbar();
      if (lane == 0) __hip_atomic_store(c.cw + CW_PROG + hm2 * 32, (unsigned)(s + 1), RLX_AGENT);
    }
    if (iskk) {
      reduce(NSEG - 1);
      vdrain();
      if (lane == 0) __hip_atomic_store(c.cw + CW_PROG + hm2 * 32, (unsigned)(NSEG + 1), RLX_AGENT);
    }
    return;
  }
  v4u wt[2][14];
#pragma unroll
  for (int t2 = 0; t2 < 2; ++t2)
#pragma unroll
    for (int ks = 0; ks < 14; ++ks) wt[t2][ks] = *(const v4u*)(wf + ((size_t)((m * 28 + 14 * half + 2 * w + t2) * 14 + ks) * 64 + lane) * 16);
  for (int s = 0; s < NSEG; ++s) {
    lbar();
    const unsigned char* xb = lds + (s & 1) * XB;
    f32x4 acc[2][2];
#pragma unroll
    for (int a = 0; a < 2; ++a) { acc[a][0] = f32x4{0, 0, 0, 0}; acc[a][1] = f32x4{0, 0, 0, 0}; }
    if (iskk) {
#pragma unroll
      for (int ks = 0; ks < 14; ++ks)
#pragma unroll
        for (int rt = 0; rt < 2; ++rt) {
          v4u xf = *(const v4u*)(xb + (rt * 14 + ks) * 1024 + lane * 16);
          acc[rt][0] = mfma(wt[0][ks], xf, acc[rt][0]); acc[rt][1] = mfma(wt[1][ks], xf, acc[rt][1]);
        }
      const int gq2 = (gq >> 1) + 2 * (gq & 1);
      v4u kf[2];
#pragma unroll
      for (int rt = 0; rt < 2; ++rt) {
        unsigned x0 = pk2(acc[rt][0][0], acc[rt][0][1]), x1 = pk2(acc[rt][0][2], acc[rt][0][3]);
        unsigned y0 = pk2(acc[rt][1][0], acc[rt][1][1]), y1 = pk2(acc[rt][1][2], acc[rt][1][3]);
        auto s0 = __builtin_amdgcn_permlane16_swap(x0, y0, false, false);
        auto s1 = __builtin_amdgcn_permlane16_swap(x1, y1, false, false);
        const int g = c16 >> 2, rowq = (c16 & 3) + 4 * rt;
        kf[rt] = v4u{s0[0], s1[0], s0[1], s1[1]};
        st128s(kf[rt], c.rs, (int)(KKF_OFF + ((size_t)(((s * 16 + h) * 4 + g) * 7 + w) * 64 + rowq + 8 * half + 16 * gq2) * 16));
      }
#pragma unroll
      for (int qt = 0; qt < 2; ++qt) {
        const v4u xq = *(const v4u*)(xb + (qt * 14 + 7 * half + w) * 1024 + (c16 + 16 * gq2) * 16);
#pragma unroll
        for (int kt = 0; kt < 2; ++kt)
          *(f32x4*)(lds + L_SP + (s & 1) * XB + (w * 4 + kt * 2 + qt) * 1024 + lane * 16) = mfma(kf[kt], xq, f32x4{0, 0, 0, 0});
      }
    } else {
#pragma unroll
      for (int ks = 0; ks < 14; ++ks)
#pragma unroll
        for (int rt = 0; rt < 2; ++rt) {
          v4u xf = *(const v4u*)(xb + (rt * 14 + ks) * 1024 + lane * 16);
          acc[rt][0] = mfma(xf, wt[0][ks], acc[rt][0]); acc[rt][1] = mfma(xf, wt[1][ks], acc[rt][1]);
        }
#pragma unroll
      for (int t2 = 0; t2 < 2; ++t2) {
        const int oc = 14 * half + 2 * w + t2;
        v4u o = {pk2(acc[0][t2][0], acc[0][t2][1]), pk2(acc[0][t2][2], acc[0][t2][3]), pk2(acc[1][t2][0], acc[1][t2][1]), pk2(acc[1][t2][2], acc[1][t2][3])};
        st128s(o, c.rs, (int)(VVF_OFF + ((size_t)((s * 28 + oc) * 16 + h) * 64 + lane) * 16));
      }
    }
    vdrain();
    lbar();
  }
}

DI void wait_vec(GAS unsigned* word, unsigned want, GAS unsigned* tmo) {
  for (unsigned spins = 0;; ++spins) {
    const unsigned v = __hip_atomic_load(word, RLX_AGENT);
    if (__all(v >= want)) return;
    if (spins > SPIN_CAP) { __hip_atomic_store(tmo, 1u, RLX_AGENT); return; }
    if ((spins & 255u) == 255u && __builtin_amdgcn_readfirstlane(__hip_atomic_load(tmo, RLX_AGENT))) return;
    __builtin_amdgcn_s_sleep(4);
  }
}
DI float bflo(unsigned u) { return __uint_as_float(u << 16); }
DI float bfhi(unsigned u) { return __uint_as_float(u & 0xffff0000u); }

constexpr int LT_X = 0, LT_PX = 28 * 1024, LT_PM = 60 * 1024;
DI void task_T(const Params& p, const Ctx& c, unsigned char* lds, int seg, int cg) {
  const int tid = threadIdx.x, lane = tid & 63, w = __builtin_amdgcn_readfirstlane(tid >> 6);
  const int gq = lane >> 4, c16 = lane & 15;
  GAS unsigned* tmo = c.cw + CW_TMO;
  const int oc = 7 * cg + (w < 7 ? w : 0);
#pragma unroll
  for (int i = 0; i < 4; ++i) {
    const int fr = w + 8 * i;
    if (fr < 28) { const int qt = fr / 14, ks = fr - 14 * qt; *(v4u*)(lds + LT_X + fr * 1024 + lane * 16) = *(const v4u*)(p.x + (size_t)(32 * seg + 16 * qt + c16) * D + 32 * ks + 8 * gq); }
  }
  if (w == 7) wait_vec(c.cw + CW_PROG + lane * 32, (unsigned)seg + (lane < 32 ? 2u : 1u), tmo);
  lbar();
  v4u sx[2][2][2], vv[16];
#pragma unroll
  for (int hh = 0; hh < 2; ++hh)
#pragma unroll
    for (int hf = 0; hf < 2; ++hf)
#pragma unroll
      for (int qt = 0; qt < 2; ++qt) sx[hh][hf][qt] = ld128s(c.rs, (int)(SXX_OFF + ((size_t)(((seg * 16 + 2 * w + hh) * 2 + hf) * 2 + qt) * 64 + lane) * 16));
  if (w < 7) {
#pragma unroll
    for (int h = 0; h < 16; ++h) vv[h] = ld128s(c.rs, (int)(VVF_OFF + ((size_t)((seg * 28 + oc) * 16 + h) * 64 + lane) * 16));
  } else {
    wait_ge(c.cw + CW_PM + ((cg * 16 + (seg & 15)) * 32), seg + 1, tmo);
    wait_vec(c.cw + CW_PM + lane * 32, seg + 1, tmo);
    wait_vec(c.cw + CW_PM + (64 + (lane < 28 ? lane : 0)) * 32, seg + 1, tmo);
  }
  lbar();
  v4u kf[14]; v2u vm[8];
#pragma unroll
  for (int ks = 0; ks < 14; ++ks) kf[ks] = ld128s(c.rs, (int)(KKM_OFF + (size_t)((seg * 16 + 2 * w + (c16 >> 3)) * 56 + 4 * ks + gq) * 128 + (c16 & 7) * 16));
  if (w < 7) {
#pragma unroll
    for (int pr = 0; pr < 8; ++pr) vm[pr] = ld64s(c.rs, (int)(VMF_OFF + ((size_t)((seg * 28 + oc) * 8 + pr) * 64 + lane) * 8));
  }
#pragma unroll
  for (int qt = 0; qt < 2; ++qt) {
    f32x4 am = {0, 0, 0, 0}, am2 = {0, 0, 0, 0};
#pragma unroll
    for (int ks = 0; ks < 14; ks += 2) {
      am = mfma(kf[ks], *(const v4u*)(lds + LT_X + (qt * 14 + ks) * 1024 + lane * 16), am);
      am2 = mfma(kf[ks + 1], *(const v4u*)(lds + LT_X + (qt * 14 + ks + 1) * 1024 + lane * 16), am2);
    }
    am += am2;
    const int q = 16 * qt + c16;
    float sc[2][8], mxh[2];
#pragma unroll
    for (int hh = 0; hh < 2; ++hh) {
      float m = (gq >> 1) == hh ? fmaxf(fmaxf(am[0], am[1]), fmaxf(am[2], am[3])) : -1e9f;
#pragma unroll
      for (int k = 0; k < 4; ++k) {
        const unsigned u0 = sx[hh][0][qt][k], u1 = sx[hh][1][qt][k];
        const int key = (k < 2 ? 4 * gq + 2 * k : 16 + 4 * gq + 2 * (k - 2));
        sc[hh][2 * k] = key <= q ? bflo(u0) + bflo(u1) : -1e9f;
        sc[hh][2 * k + 1] = key + 1 <= q ? bfhi(u0) + bfhi(u1) : -1e9f;
        m = fmaxf(m, fmaxf(sc[hh][2 * k], sc[hh][2 * k + 1]));
      }
      mxh[hh] = swap32_max(swap16_max(m)) * LOG2E;
    }
    float em[4], lh[2];
    const float mm = gq < 2 ? mxh[0] : mxh[1];
#pragma unroll
    for (int r = 0; r < 4; ++r) em[r] = ex2(fmaf(am[r], LOG2E, -mm));
    const float lm = (em[0] + em[1]) + (em[2] + em[3]);
#pragma unroll
    for (int hh = 0; hh < 2; ++hh) {
      float l = (gq >> 1) == hh ? lm : 0.f;
#pragma unroll
      for (int k = 0; k < 8; ++k) { sc[hh][k] = ex2(fmaf(sc[hh][k], LOG2E, -mxh[hh])); l += sc[hh][k]; }
      lh[hh] = __builtin_amdgcn_rcpf(swap32_add(swap16_add(l)));
      *(v4u*)(lds + LT_PX + ((2 * w + hh) * 2 + qt) * 1024 + lane * 16) =
          v4u{pk2(sc[hh][0] * lh[hh], sc[hh][1] * lh[hh]), pk2(sc[hh][2] * lh[hh], sc[hh][3] * lh[hh]), pk2(sc[hh][4] * lh[hh], sc[hh][5] * lh[hh]), pk2(sc[hh][6] * lh[hh], sc[hh][7] * lh[hh])};
    }
    const float rm = gq < 2 ? lh[0] : lh[1];
    *(v2u*)(lds + LT_PM + (w * 2 + qt) * 512 + lane * 8) = v2u{pk2(em[0] * rm, em[1] * rm), pk2(em[2] * rm, em[3] * rm)};
  }
  lbar();
  if (w < 7) {
    f32x4 o[2] = {f32x4{0, 0, 0, 0}, f32x4{0, 0, 0, 0}};
#pragma unroll
    for (int h = 0; h < 16; ++h)
#pragma unroll
      for (int qt = 0; qt < 2; ++qt) o[qt] = mfma(vv[h], *(const v4u*)(lds + LT_PX + (h * 2 + qt) * 1024 + lane * 16), o[qt]);
#pragma unroll
    for (int i = 0; i < 4; ++i)
#pragma unroll
      for (int qt = 0; qt < 2; ++qt) {
        const v2u b0 = *(const v2u*)(lds + LT_PM + ((2 * i) * 2 + qt) * 512 + lane * 8), b1 = *(const v2u*)(lds + LT_PM + ((2 * i + 1) * 2 + qt) * 512 + lane * 8);
        o[qt] = mfma(v4u{vm[2 * i].x, vm[2 * i].y, vm[2 * i + 1].x, vm[2 * i + 1].y}, v4u{b0.x, b0.y, b1.x, b1.y}, o[qt]);
      }
#pragma unroll
    for (int qt = 0; qt < 2; ++qt)
      *(v2u*)(p.out + (size_t)(32 * seg + 16 * qt + c16) * (2 * D) + 16 * oc + 4 * gq) = v2u{pk2(o[qt][0], o[qt][1]), pk2(o[qt][2], o[qt][3])};
  }
  lbar();
}

DI unsigned phi2(float a, float b) {
  const unsigned t = pk2(a, b);
  const float x = bflo(t), y = bfhi(t);
  const unsigned u = pk2(x > 0.f ? x + 1.f : __expf(x), y > 0.f ? y + 1.f : __expf(y));
  return pk2(bflo(u) + 1e-6f, bfhi(u) + 1e-6f);
}
constexpr int LB_W = 0, LB_R = 448 * 192;
DI v4u wfrag(const unsigned char* lds, int off) {
  typedef short v4s __attribute__((ext_vector_type(4)));
  v4s lo = __builtin_amdgcn_ds_read_tr16_b64_v4i16((LAS v4s*)((LAS unsigned char*)lds + off));
  v4s hi = __builtin_amdgcn_ds_read_tr16_b64_v4i16((LAS v4s*)((LAS unsigned char*)lds + off + 4 * 192));
  v2u l2 = __builtin_bit_cast(v2u, lo), h2 = __builtin_bit_cast(v2u, hi);
  return v4u{l2.x, l2.y, h2.x, h2.y};
}
DI void task_B1(const Params& p, const Ctx& c, unsigned char* lds, int h, int tq) {
  const int tid = threadIdx.x, lane = tid & 63, w = __builtin_amdgcn_readfirstlane(tid >> 6);
  const int gq = lane >> 4, c16 = lane & 15;
#pragma unroll 7
  for (int i = 0; i < 21; ++i) {
    const int idx = tid + 512 * i, k = idx / 24, rem = idx - k * 24, m = rem >> 3, sl = rem & 7;
    v2u v = {0u, 0u};
    if (sl < 7) v = *(const v2u*)(p.wqkv + (size_t)k * (3 * D) + m * D + h * 28 + sl * 4);
    *(v2u*)(lds + LB_W + k * 192 + m * 64 + sl * 8) = v;
  }
  if (w < 7) {
    const int nt = 7 * tq + w;
    unsigned e8[8];
#pragma unroll
    for (int i = 0; i < 8; ++i) { const int e = (i < 4 ? 4 * gq + i : 12 + 4 * gq + i); e8[i] = e < 28 ? (unsigned)p.wo[(size_t)(28 * h + e) * D + 16 * nt + c16] : 0u; }
    st128s(v4u{e8[0] | e8[1] << 16, e8[2] | e8[3] << 16, e8[4] | e8[5] << 16, e8[6] | e8[7] << 16}, c.rs, (int)(WOF_OFF + ((size_t)(h * 28 + nt) * 64 + lane) * 16));
  }
  lbar();
  const int t0 = 512 * tq + 64 * w;
  const u16* xr = p.bx + (size_t)(t0 + c16) * D + 8 * gq;
  const int wb = LB_W + (8 * gq + (c16 >> 2)) * 192 + (lane & 3) * 8;
  f32x4 aq[4][2], ak[4][2], av[4][2];
#pragma unroll
  for (int rt = 0; rt < 4; ++rt)
#pragma unroll
    for (int dt = 0; dt < 2; ++dt) { aq[rt][dt] = f32x4{0, 0, 0, 0}; ak[rt][dt] = f32x4{0, 0, 0, 0}; av[rt][dt] = f32x4{0, 0, 0, 0}; }
  v4u xn[4];
#pragma unroll
  for (int rt = 0; rt < 4; ++rt) xn[rt] = *(const v4u*)(xr + (size_t)(16 * rt) * D);
#pragma unroll 1
  for (int ks = 0; ks < 14; ++ks) {
    v4u x[4];
#pragma unroll
    for (int rt = 0; rt < 4; ++rt) x[rt] = xn[rt];
    const int kn = ks + 1 < 14 ? ks + 1 : ks;
#pragma unroll
    for (int rt = 0; rt < 4; ++rt) xn[rt] = *(const v4u*)(xr + (size_t)(16 * rt) * D + 32 * kn);
#pragma unroll
    for (int dt = 0; dt < 2; ++dt) {
      const v4u wq = wfrag(lds, wb + ks * 32 * 192 + dt * 32), wk = wfrag(lds, wb + ks * 32 * 192 + 64 + dt * 32), wv = wfrag(lds, wb + ks * 32 * 192 + 128 + dt * 32);
#pragma unroll
      for (int rt = 0; rt < 4; ++rt) { aq[rt][dt] = mfma(wq, x[rt], aq[rt][dt]); ak[rt][dt] = mfma(x[rt], wk, ak[rt][dt]); av[rt][dt] = mfma(x[rt], wv, av[rt][dt]); }
    }
  }
#pragma unroll
  for (int rt = 0; rt < 4; ++rt)
    st128s(v4u{phi2(aq[rt][0][0], aq[rt][0][1]), phi2(aq[rt][0][2], aq[rt][0][3]), phi2(aq[rt][1][0], aq[rt][1][1]), phi2(aq[rt][1][2], aq[rt][1][3])}, c.rs,
           (int)(PQ_OFF + ((size_t)((t0 / 16 + rt) * 16 + h) * 64 + lane) * 16));
  f32x4 kv[2][2] = {{f32x4{0, 0, 0, 0}, f32x4{0, 0, 0, 0}}, {f32x4{0, 0, 0, 0}, f32x4{0, 0, 0, 0}}};
#pragma unroll
  for (int rp = 0; rp < 2; ++rp) {
    v4u pk[2], vf[2];
#pragma unroll
    for (int dt = 0; dt < 2; ++dt) {
      const f32x4 k0 = ak[2 * rp][dt], k1 = ak[2 * rp + 1][dt], v0 = av[2 * rp][dt], v1 = av[2 * rp + 1][dt];
      pk[dt] = v4u{phi2(k0[0], k0[1]), phi2(k0[2], k0[3]), phi2(k1[0], k1[1]), phi2(k1[2], k1[3])};
      vf[dt] = v4u{pk2(v0[0], v0[1]), pk2(v0[2], v0[3]), pk2(v1[0], v1[1]), pk2(v1[2], v1[3])};
    }
    if (c16 >= 12) pk[1] = v4u{0u, 0u, 0u, 0u};
    if (c16 == 12) vf[1] = v4u{0x3F803F80u, 0x3F803F80u, 0x3F803F80u, 0x3F803F80u};
#pragma unroll
    for (int dt = 0; dt < 2; ++dt)
#pragma unroll
      for (int et = 0; et < 2; ++et) kv[dt][et] = mfma(pk[dt], vf[et], kv[dt][et]);
  }
#pragma unroll
  for (int t = 0; t < 4; ++t) *(f32x4*)(lds + LB_R + (w * 4 + t) * 1024 + lane * 16) = kv[t >> 1][t & 1];
  lbar();
  if (w < 4) {
    f32x4 a = *(const f32x4*)(lds + LB_R + w * 1024 + lane * 16);
#pragma unroll
    for (int w2 = 1; w2 < 8; ++w2) a += *(const f32x4*)(lds + LB_R + (w2 * 4 + w) * 1024 + lane * 16);
    st128s(__builtin_bit_cast(v4u, a), c.rs, (int)(KVP_OFF + ((size_t)((h * 4 + tq) * 4 + w) * 64 + lane) * 16));
  }
  vdrain();
  lbar();
  if (tid == 0) __hip_atomic_store(c.cw + CW_B1 + h * 4 + tq, 1u, RLX_AGENT);
}

constexpr int LB_KVA = 0, LB_OF = 32 * 1024;
DI void task_B2(const Params& p, const Ctx& c, unsigned char* lds, int tt) {
  const int tid = threadIdx.x, lane = tid & 63, w = __builtin_amdgcn_readfirstlane(tid >> 6);
  const int gq = lane >> 4, c16 = lane & 15;
  if (w == 0) wait_vec(c.cw + CW_B1 + lane, 1u, c.cw + CW_TMO);
  lbar();
  v4u pq[2][4];
#pragma unroll
  for (int hh = 0; hh < 2; ++hh)
#pragma unroll
    for (int t = 0; t < 4; ++t) pq[hh][t] = ld128s(c.rs, (int)(PQ_OFF + ((size_t)((4 * tt + t) * 16 + 2 * w + hh) * 64 + lane) * 16));
  v4u kva[2][2];
#pragma unroll
  for (int hh = 0; hh < 2; ++hh)
#pragma unroll
    for (int et = 0; et < 2; ++et) {
      f32x4 d0 = {0, 0, 0, 0}, d1 = {0, 0, 0, 0};
#pragma unroll
      for (int tq = 0; tq < 4; ++tq) {
        d0 += __builtin_bit_cast(f32x4, ld128s(c.rs, (int)(KVP_OFF + ((size_t)(((2 * w + hh) * 4 + tq) * 4 + et) * 64 + lane) * 16)));
        d1 += __builtin_bit_cast(f32x4, ld128s(c.rs, (int)(KVP_OFF + ((size_t)(((2 * w + hh) * 4 + tq) * 4 + 2 + et) * 64 + lane) * 16)));
      }
      kva[hh][et] = v4u{pk2(d0[0], d0[1]), pk2(d0[2], d0[3]), pk2(d1[0], d1[1]), pk2(d1[2], d1[3])};
    }
#pragma unroll
  for (int hh = 0; hh < 2; ++hh)
#pragma unroll
    for (int t = 0; t < 4; ++t) {
      const f32x4 n0 = mfma(kva[hh][0], pq[hh][t], f32x4{0, 0, 0, 0}), n1 = mfma(kva[hh][1], pq[hh][t], f32x4{0, 0, 0, 0});
      float den = swap32_add(swap16_add(gq == 3 ? n1[0] : 0.f));
      den = bflo(pk2(bflo(pk2(den, 0.f)) + 1e-6f, 0.f));
      const float rd = 1.f / den;
      const unsigned a0 = pk2(n0[0], n0[1]), a1 = pk2(n0[2], n0[3]), b0 = pk2(n1[0], n1[1]), b1 = pk2(n1[2], n1[3]);
      v4u o = {pk2(bflo(a0) * rd, bfhi(a0) * rd), pk2(bflo(a1) * rd, bfhi(a1) * rd), pk2(bflo(b0) * rd, bfhi(b0) * rd), pk2(bflo(b1) * rd, bfhi(b1) * rd)};
      if (gq == 3) { o.z = 0u; o.w = 0u; }
      *(v4u*)(lds + LB_OF + ((2 * w + hh) * 4 + t) * 1024 + lane * 16) = o;
    }
  lbar();
  f32x4 acc[4][4];
#pragma unroll
  for (int j = 0; j < 4; ++j)
#pragma unroll
    for (int t = 0; t < 4; ++t) acc[j][t] = f32x4{0, 0, 0, 0};
  const int nj = w < 4 ? 4 : 3;
#pragma unroll 2
  for (int h = 0; h < 16; ++h) {
    v4u a[4], b[4];
#pragma unroll
    for (int j = 0; j < 4; ++j) a[j] = ld128s(c.rs, (int)(WOF_OFF + ((size_t)(h * 28 + (j < nj ? w + 8 * j : w)) * 64 + lane) * 16));
#pragma unroll
    for (int t = 0; t < 4; ++t) b[t] = *(const v4u*)(lds + LB_OF + (h * 4 + t) * 1024 + lane * 16);
#pragma unroll
    for (int j = 0; j < 4; ++j)
#pragma unroll
      for (int t = 0; t < 4; ++t) acc[j][t] = mfma(a[j], b[t], acc[j][t]);
  }
#pragma unroll
  for (int j = 0; j < 4; ++j)
    if (j < nj) {
#pragma unroll
      for (int t = 0; t < 4; ++t)
        *(v2u*)(p.out + (size_t)(64 * tt + 16 * t + c16) * (2 * D) + D + 16 * (w + 8 * j) + 4 * gq) = v2u{pk2(acc[j][t][0], acc[j][t][1]), pk2(acc[j][t][2], acc[j][t][3])};
    }
  lbar();
}

DI void role_W(const Params& p, const Ctx& c, unsigned char* lds, int wid) {
  task_B1(p, c, lds, wid >> 2, wid & 3);
  if ((wid & 1) == 0) task_B2(p, c, lds, wid >> 1);
#pragma unroll 1
  for (int k = 0; k < 4; ++k) task_T(p, c, lds, 16 * k + (wid >> 2), wid & 3);
}

__global__ void __launch_bounds__(512, 2) k_main(Params p) {
  extern __shared__ __attribute__((aligned(16))) unsigned char lds[];
  const int b = blockIdx.x, label = b & 7, rank = b >> 3;
  Ctx c;
  c.rs = __builtin_amdgcn_make_buffer_rsrc(p.ws, 0, (int)WS_USED, 0x00020000);
  c.cw = (GAS unsigned*)(p.ws + CTRL_OFF);
  c.cls = __builtin_amdgcn_readfirstlane(((const unsigned*)(p.ws + CTRL_OFF))[CW_CLASS] & 1u);
  if (label < 2) { const int id = rank * 2 + label; role_S(p, c, lds, id >> 2, id & 3); }
  else if (label == 2) { if (rank < 28) role_O(p, c, lds, rank); }
  else if (label == 4 || label == 5) role_KV(p, c, lds, rank * 2 + (label - 4));
  else if (label >= 6) role_W(p, c, lds, rank * 2 + (label - 6));
}

static unsigned char* align_ws(void* d_ws) { return (unsigned char*)(((uintptr_t)d_ws + (2u << 20) - 1) & ~(uintptr_t)((2u << 20) - 1)); }
static void launch_v7(void* const* d_in, void* d_out, void* d_ws, hipStream_t stream) {
  Params p;
  p.x = (const u16*)d_in[0]; p.mem0 = (const u16*)d_in[1]; p.Wq = (const u16*)d_in[2]; p.Wk = (const u16*)d_in[3]; p.Wv = (const u16*)d_in[4]; p.Wo = (const u16*)d_in[5];
  p.bx = (const u16*)d_in[7]; p.wqkv = (const u16*)d_in[8]; p.wo = (const u16*)d_in[9];
  p.out = (u16*)d_out; p.ws = align_ws(d_ws);
  hipFuncSetAttribute((const void*)k_prep, hipFuncAttributeMaxDynamicSharedMemorySize, 64 * 1024);
  hipFuncSetAttribute((const void*)k_main, hipFuncAttributeMaxDynamicSharedMemorySize, 128 * 1024);
  k_prep<<<258, 512, 60 * 1024, stream>>>(p);
  k_main<<<256, 512, 128 * 1024, stream>>>(p);
}
extern "C" void kernel_launch(void* const* d_in, const int* in_sizes, int n_in, void* d_out, int out_size, void* d_ws, size_t ws_size,
                              hipStream_t stream) {
  (void)in_sizes; (void)n_in; (void)out_size; (void)ws_size;
  launch_v7(d_in, d_out, d_ws, stream);
}
```

```cpp
#include <hip/hip_runtime.h>
#include <hip/hip_bf16.h>
#include <stdint.h>

typedef unsigned short u16;
typedef unsigned long long u64;
typedef unsigned v4u __attribute__((ext_vector_type(4)));
typedef unsigned v2u __attribute__((ext_vector_type(2)));
typedef float f32x4 __attribute__((ext_vector_type(4)));
typedef float f32x2 __attribute__((ext_vector_type(2)));
typedef short bf16x8 __attribute__((ext_vector_type(8)));
typedef __bf16 bf16x2_t __attribute__((ext_vector_type(2)));
#define GAS __attribute__((address_space(1)))
#define LAS __attribute__((address_space(3)))
#define DI __device__ __forceinline__
#define RLX_AGENT __ATOMIC_RELAXED, __HIP_MEMORY_SCOPE_AGENT

constexpr int D = 448, H = 16, E = 256, NSEG = 64;
constexpr unsigned SENT = 0xFFFFFFFFu;
constexpr int AUX_SC1 = 16;
constexpr float LOG2E = 1.4426950408889634f;
constexpr unsigned SPIN_CAP = 1u << 20;

constexpr size_t XCH_OFF = 0;
constexpr size_t CTRL_OFF = 1u << 20;
constexpr size_t WF_OFF = 2u << 20;
constexpr size_t KKF_OFF = 16u << 20;
constexpr size_t VVF_OFF = 46u << 20;
constexpr size_t KKM_OFF = 76u << 20;
constexpr size_t VMF_OFF = 84u << 20;
constexpr size_t SXX_OFF = 92u << 20;
constexpr size_t KVP_OFF = 97u << 20;
constexpr size_t WOF_OFF = 98u << 20;
constexpr size_t PQ_OFF = 100u << 20;
constexpr size_t WS_USED = 128u << 20;
constexpr int CW_EPOCH = 0;
constexpr int CW_CLASS = 16;
constexpr int CW_TMO = 32;
constexpr int CW_PF = 64;
constexpr int CW_B1 = CW_PF + 256;
constexpr int CW_PROG = CW_B1 + 64;
constexpr int CW_PM = CW_PROG + 64 * 16;

DI unsigned pk2(float a, float b) { f32x2 v = {a, b}; bf16x2_t r = __builtin_convertvector(v, bf16x2_t); return __builtin_bit_cast(unsigned, r); }
DI f32x4 mfma(v4u a, v4u b, f32x4 c) { return __builtin_amdgcn_mfma_f32_16x16x32_bf16(__builtin_bit_cast(bf16x8, a), __builtin_bit_cast(bf16x8, b), c, 0, 0, 0); }
DI v4u ld128s(__amdgpu_buffer_rsrc_t rs, int off) { return __builtin_amdgcn_raw_buffer_load_b128(rs, off, 0, AUX_SC1); }
DI v2u ld64s(__amdgpu_buffer_rsrc_t rs, int off) { return __builtin_amdgcn_raw_buffer_load_b64(rs, off, 0, AUX_SC1); }
DI unsigned ld32s(__amdgpu_buffer_rsrc_t rs, int off) { return __builtin_amdgcn_raw_buffer_load_b32(rs, off, 0, AUX_SC1); }
DI void st128s(v4u v, __amdgpu_buffer_rsrc_t rs, int off) { __builtin_amdgcn_raw_buffer_store_b128(v, rs, off, 0, AUX_SC1); }
DI void st64s(v2u v, __amdgpu_buffer_rsrc_t rs, int off) { __builtin_amdgcn_raw_buffer_store_b64(v, rs, off, 0, AUX_SC1); }
DI void st32s(unsigned v, __amdgpu_buffer_rsrc_t rs, int off) { __builtin_amdgcn_raw_buffer_store_b32(v, rs, off, 0, AUX_SC1); }
DI unsigned canon(unsigned x) { return x < 0xFFFFFFFEu ? x : 0xFFFFFFFEu; }
DI unsigned umax(unsigned a, unsigned b) { return a > b ? a : b; }
DI unsigned mx4(v4u v) { return umax(umax(v.x, v.y), umax(v.z, v.w)); }
DI bool ok4(v4u v) { return mx4(v) != SENT; }
DI void lbar() { asm volatile("s_waitcnt lgkmcnt(0)\n\ts_barrier" ::: "memory"); }
DI void vdrain() { asm volatile("s_waitcnt vmcnt(0)" ::: "memory"); }
DI float swap32_add(float x) { auto r = __builtin_amdgcn_permlane32_swap(__float_as_uint(x), __float_as_uint(x), false, false); return __uint_as_float(r[0]) + __uint_as_float(r[1]); }
DI float swap16_add(float x) { auto r = __builtin_amdgcn_permlane16_swap(__float_as_uint(x), __float_as_uint(x), false, false); return __uint_as_float(r[0]) + __uint_as_float(r[1]); }
DI float swap32_max(float x) { auto r = __builtin_amdgcn_permlane32_swap(__float_as_uint(x), __float_as_uint(x), false, false); return fmaxf(__uint_as_float(r[0]), __uint_as_float(r[1])); }
DI float swap16_max(float x) { auto r = __builtin_amdgcn_permlane16_swap(__float_as_uint(x), __float_as_uint(x), false, false); return fmaxf(__uint_as_float(r[0]), __uint_as_float(r[1])); }
DI float ror8(float x) { return __uint_as_float((unsigned)__builtin_amdgcn_update_dpp(0, (int)__float_as_uint(x), 0x128, 0xf, 0xf, false)); }
DI float ex2(float x) { return __builtin_amdgcn_exp2f(x); }
DI float bflo(unsigned u) { return __uint_as_float(u << 16); }
DI float bfhi(unsigned u) { return __uint_as_float(u & 0xffff0000u); }
DI int xch_off(int slot, int blk, int cls) { return ((slot * 5 + blk) * 2 + cls) * 8192; }

struct Params {
  const u16* x; const u16* mem0; const u16* Wq; const u16* Wk; const u16* Wv; const u16* Wo;
  const u16* bx; const u16* wqkv; const u16* wo;
  u16* out; unsigned char* ws;
};
struct Ctx { __amdgpu_buffer_rsrc_t rs; GAS u64* cw; u64 tagw; int cls; };

DI void cw_set(const Ctx& c, int idx, unsigned n) { __hip_atomic_store(c.cw + idx, c.tagw | n, RLX_AGENT); }
DI bool cw_ge(const Ctx& c, u64 v, unsigned want) { const u64 d = v - c.tagw; return d <= 255ull && (unsigned)d >= want; }
DI bool cw_tmo(const Ctx& c) { return __builtin_amdgcn_readfirstlane((int)cw_ge(c, __hip_atomic_load(c.cw + CW_TMO, RLX_AGENT), 1u)) != 0; }
DI bool wait_vec(const Ctx& c, int idx, unsigned want, int nap) {
  for (unsigned spins = 0;; ++spins) {
    const u64 v = __hip_atomic_load(c.cw + idx, RLX_AGENT);
    if (__all(cw_ge(c, v, want))) return true;
    if (spins > SPIN_CAP) { cw_set(c, CW_TMO, 1u); return false; }
    if ((spins & 255u) == 255u && cw_tmo(c)) return false;
    if (nap == 1) __builtin_amdgcn_s_sleep(1); else __builtin_amdgcn_s_sleep(4);
  }
}
DI bool poll128(const Ctx& c, int off, v4u& m) {
  unsigned spins = 0;
  bool good = true;
  do {
    asm volatile("" ::: "memory");
    m = ld128s(c.rs, off);
    if (++spins > SPIN_CAP) { good = false; break; }
  } while (!ok4(m));
  if (!__all(good)) { cw_set(c, CW_TMO, 1u); return false; }
  return true;
}

template <bool ISA>
DI void prep_block(const Params& p, const Ctx& c, unsigned char* lds, int m, int ra, int cb, bool docal, unsigned (&best)[2]) {
  const int tid = threadIdx.x, lane = tid & 63, w = __builtin_amdgcn_readfirstlane(tid >> 6);
  const int h = m & 15, r16 = lane & 15, gq = lane >> 4;
  constexpr int RS = 272, L_R = 0, L_L = 224 * RS;
  const u16* rowsrc = ISA ? p.Wk : p.Wv;
  v4u gr[2][7], gl[2][4];
#pragma unroll
  for (int hf = 0; hf < 2; ++hf) {
#pragma unroll
    for (int i = 0; i < 7; ++i) { const int q = tid + 512 * i; gr[hf][i] = *(const v4u*)(rowsrc + ((size_t)(224 * cb + (q >> 4)) * H + h) * E + 128 * hf + (q & 15) * 8); }
#pragma unroll
    for (int i = 0; i < 4; ++i) {
      const int q = (i < 3 || tid < 256) ? tid + 512 * i : tid;
      if constexpr (ISA) gl[hf][i] = *(const v4u*)(p.Wq + ((size_t)(112 * ra + (q >> 4)) * H + h) * E + 128 * hf + (q & 15) * 8);
      else { const int e = q / 14, ch = q - e * 14; gl[hf][i] = *(const v4u*)(p.Wo + ((size_t)h * E + 128 * hf + e) * D + 112 * ra + ch * 8); }
    }
  }
  f32x4 acc[7][2];
#pragma unroll
  for (int t = 0; t < 7; ++t) { acc[t][0] = f32x4{0, 0, 0, 0}; acc[t][1] = f32x4{0, 0, 0, 0}; }
#pragma unroll
  for (int hf = 0; hf < 2; ++hf) {
    if (hf) lbar();
#pragma unroll
    for (int i = 0; i < 7; ++i) { const int q = tid + 512 * i; *(v4u*)(lds + L_R + (q >> 4) * RS + (q & 15) * 16) = gr[hf][i]; }
#pragma unroll
    for (int i = 0; i < 4; ++i) {
      const int q = (i < 3 || tid < 256) ? tid + 512 * i : tid;
      if constexpr (ISA) *(v4u*)(lds + L_L + (q >> 4) * RS + (q & 15) * 16) = gl[hf][i];
      else { const int e = q / 14, ch = q - e * 14; *(v4u*)(lds + L_L + e * 224 + ch * 16) = gl[hf][i]; }
    }
    lbar();
    if (w < 7) {
      v4u fa[2][2], fb[2][7];
      auto rd = [&](int es, int bf) {
        fa[bf][0] = *(const v4u*)(lds + L_R + (32 * w + r16) * RS + es * 64 + gq * 16); fa[bf][1] = *(const v4u*)(lds + L_R + (32 * w + 16 + r16) * RS + es * 64 + gq * 16);
#pragma unroll
        for (int t = 0; t < 7; ++t) {
          if constexpr (ISA) fb[bf][t] = *(const v4u*)(lds + L_L + (16 * t + r16) * RS + es * 64 + gq * 16);
          else {
            const int q = (lane & 15) >> 2, pp = lane & 3;
            const int base = L_L + (32 * es + 8 * gq + q) * 224 + (16 * t + 4 * pp) * 2;
            typedef short v4s __attribute__((ext_vector_type(4)));
            v4s lo = __builtin_amdgcn_ds_read_tr16_b64_v4i16((LAS v4s*)((LAS unsigned char*)lds + base));
            v4s hi = __builtin_amdgcn_ds_read_tr16_b64_v4i16((LAS v4s*)((LAS unsigned char*)lds + base + 4 * 224));
            v2u l2 = __builtin_bit_cast(v2u, lo), h2 = __builtin_bit_cast(v2u, hi);
            fb[bf][t] = v4u{l2.x, l2.y, h2.x, h2.y};
          }
        }
      };
      rd(0, 0);
#pragma unroll
      for (int es = 0; es < 4; ++es) {
        __builtin_amdgcn_sched_barrier(0);
        if (es < 3) rd(es + 1, (es + 1) & 1);
        __builtin_amdgcn_sched_barrier(0);
#pragma unroll
        for (int t = 0; t < 7; ++t) { acc[t][0] = mfma(fa[es & 1][0], fb[es & 1][t], acc[t][0]); acc[t][1] = mfma(fa[es & 1][1], fb[es & 1][t], acc[t][1]); }
      }
      __builtin_amdgcn_sched_barrier(0);
    } else if (docal) {
#pragma unroll 1
      for (int rep = 0; rep < 6; ++rep) {
        const int cls = rep & 1;
        vdrain();
        const unsigned long long t0 = __builtin_amdgcn_s_memtime();
        const unsigned x = ld32s(c.rs, (int)XCH_OFF + (40 + 2 * (3 * hf + (rep >> 1)) + cls) * 8192);
        asm volatile("s_waitcnt vmcnt(0)" :: "v"(x) : "memory");
        const unsigned d = (unsigned)(__builtin_amdgcn_s_memtime() - t0);
        if (cls) best[1] = d < best[1] ? d : best[1]; else best[0] = d < best[0] ? d : best[0];
      }
    }
  }
  if (w < 7) {
    const float sc = ISA ? 0.0625f : 1.0f;
    const int gq2 = (gq >> 1) + 2 * (gq & 1);
#pragma unroll
    for (int t = 0; t < 7; ++t) {
      unsigned x0 = pk2(acc[t][0][0] * sc, acc[t][0][1] * sc), x1 = pk2(acc[t][0][2] * sc, acc[t][0][3] * sc);
      unsigned y0 = pk2(acc[t][1][0] * sc, acc[t][1][1] * sc), y1 = pk2(acc[t][1][2] * sc, acc[t][1][3] * sc);
      auto s0 = __builtin_amdgcn_permlane16_swap(x0, y0, false, false);
      auto s1 = __builtin_amdgcn_permlane16_swap(x1, y1, false, false);
      v4u o = {s0[0], s1[0], s0[1], s1[1]};
      st128s(o, c.rs, (int)(WF_OFF + ((size_t)((m * 28 + 7 * ra + t) * 14 + 7 * cb + w) * 64 + r16 + 16 * gq2) * 16));
    }
  }
}
DI v4u ld_wf(const Ctx& c, int tile, int ks, int lane) { return ld128s(c.rs, (int)(WF_OFF + ((size_t)(tile * 14 + ks) * 64 + lane) * 16)); }
DI v4u ld_mem0(const Params& p, int k, int lane) { return *(const v4u*)(p.mem0 + (lane & 7) * D + 32 * (k + 7 * ((lane >> 3) & 1)) + 8 * (lane >> 4)); }
DI bool wait_all_prep(Ctx& c, int lane) {
  bool ok = true;
#pragma unroll
  for (int k = 0; k < 4; ++k) ok = ok && wait_vec(c, CW_PF + 64 * k + lane, 1u, 1);
  return ok;
}
DI bool wait_class(Ctx& c) {
  const bool ok = wait_vec(c, CW_CLASS, 1u, 1);
  c.cls = __builtin_amdgcn_readfirstlane((int)(__hip_atomic_load(c.cw + CW_CLASS, RLX_AGENT) >> 1) & 1);
  return ok;
}

constexpr int L_MEM = 0;
constexpr int L_C = 8192;
constexpr int L_P = 8192;

DI void role_S(const Params& p, Ctx& c, unsigned char* lds, int h, int g) {
  const int tid = threadIdx.x, lane = tid & 63, w = __builtin_amdgcn_readfirstlane(tid >> 6);
  const int gq = lane >> 4, c16 = lane & 15;
  const int rec = (h * 4 + g) * 512;
  const int recblk = 1 + (rec >> 13), recin = rec & 8191;
  bool alive = true;
  if (w < 4) {
    alive = wait_vec(c, CW_PF + h * 8 + 2 * g + (lane & 1), 1u, 1);
    v4u wa[2][14];
#pragma unroll
    for (int t2 = 0; t2 < 2; ++t2)
#pragma unroll
      for (int ks = 0; ks < 14; ++ks) {
        const int tile = (w == 3 && t2 == 1) ? 6 : 2 * w + t2;
        wa[t2][ks] = ld_wf(c, h * 28 + 7 * g + tile, ks, lane);
      }
    int maddr[2];
#pragma unroll
    for (int t2 = 0; t2 < 2; ++t2) {
      const int tile = (w == 3 && t2 == 1) ? 6 : 2 * w + t2;
      const int a = 112 * g + 16 * tile + 4 * gq, ks = a >> 5, ksq = ks >= 7 ? ks - 7 : ks, hf = ks >= 7 ? 1 : 0;
      maddr[t2] = L_MEM + ksq * 1024 + (((a & 31) >> 3) * 16 + hf * 8 + (c16 & 7)) * 16 + (a & 7) * 2;
    }
    vdrain();
    for (int s = 0; s < NSEG; ++s) {
      vdrain();
      lbar();
      v4u mf[7];
#pragma unroll
      for (int k = 0; k < 7; ++k) mf[k] = *(const v4u*)(lds + L_MEM + k * 1024 + lane * 16);
      const v2u mb0 = *(const v2u*)(lds + maddr[0]), mb1 = *(const v2u*)(lds + maddr[1]);
      float v0[4], v1[4];
      f32x4 d2;
      if (w < 3) {
        f32x4 a1 = {0, 0, 0, 0}, a2 = {0, 0, 0, 0}, b1 = {0, 0, 0, 0}, b2 = {0, 0, 0, 0};
#pragma unroll
        for (int k = 0; k < 7; ++k) {
          a1 = mfma(wa[0][k], mf[k], a1); a2 = mfma(wa[0][k + 7], mf[k], a2);
          b1 = mfma(wa[1][k], mf[k], b1); b2 = mfma(wa[1][k + 7], mf[k], b2);
        }
#pragma unroll
        for (int r = 0; r < 4; ++r) { v0[r] = (lane & 8) ? a2[r] : a1[r]; v1[r] = (lane & 8) ? b2[r] : b1[r]; }
        d2 = mfma(v4u{pk2(v0[0], v0[1]), pk2(v0[2], v0[3]), pk2(v1[0], v1[1]), pk2(v1[2], v1[3])}, v4u{mb0.x, mb0.y, mb1.x, mb1.y}, f32x4{0, 0, 0, 0});
      } else {
        f32x4 a1 = {0, 0, 0, 0}, a2 = {0, 0, 0, 0};
#pragma unroll
        for (int k = 0; k < 7; ++k) { a1 = mfma(wa[0][k], mf[k], a1); a2 = mfma(wa[0][k + 7], mf[k], a2); }
#pragma unroll
        for (int r = 0; r < 4; ++r) { v0[r] = (lane & 8) ? a2[r] : a1[r]; v1[r] = 0.f; }
        d2 = mfma(v4u{pk2(v0[0], v0[1]), pk2(v0[2], v0[3]), 0u, 0u}, v4u{mb0.x, mb0.y, 0u, 0u}, f32x4{0, 0, 0, 0});
      }
      {
        f32x4 o;
#pragma unroll
        for (int r = 0; r < 4; ++r) o[r] = swap32_add(d2[r]);
        if (c16 < 8 && gq < 2) *(f32x4*)(lds + L_P + ((w * 2 + gq) * 8 + c16) * 16) = o;
      }
      lbar();
      {
        const int cb = (s * 16 + h) * 56 + 14 * g + 4 * w + (gq >> 1);
        float f[4];
#pragma unroll
        for (int r = 0; r < 4; ++r) f[r] = v0[r] + ror8(v0[r]);
        if (c16 < 8) st64s(v2u{pk2(f[0], f[1]), pk2(f[2], f[3])}, c.rs, (int)(KKM_OFF + (size_t)cb * 128 + c16 * 16 + (gq & 1) * 8));
        if (w < 3) {
#pragma unroll
          for (int r = 0; r < 4; ++r) f[r] = v1[r] + ror8(v1[r]);
          if (c16 < 8) st64s(v2u{pk2(f[0], f[1]), pk2(f[2], f[3])}, c.rs, (int)(KKM_OFF + (size_t)(cb + 2) * 128 + c16 * 16 + (gq & 1) * 8));
        }
      }
      if (!alive) break;
    }
    vdrain();
    lbar();
  } else {
    const int u = w - 4;
    unsigned kkprog = 0;
    v4u kf[7];
    auto load_kk = [&](int s) -> bool {
      if (kkprog < (unsigned)s + 1) {
        if (!wait_vec(c, CW_PROG + (2 * h + (lane & 1)) * 16, s + 1, 1)) return false;
        kkprog = s + 1;
      }
#pragma unroll
      for (int k = 0; k < 7; ++k) kf[k] = ld128s(c.rs, (int)(KKF_OFF + ((size_t)(((s * 16 + h) * 4 + g) * 7 + k) * 64 + lane) * 16));
      return true;
    };
    {
      const v4u m0 = ld_mem0(p, 2 * u, lane), m1 = ld_mem0(p, u < 3 ? 2 * u + 1 : 6, lane);
      *(v4u*)(lds + L_MEM + (2 * u) * 1024 + lane * 16) = m0;
      if (u < 3) *(v4u*)(lds + L_MEM + (2 * u + 1) * 1024 + lane * 16) = m1;
    }
    alive = wait_all_prep(c, lane);
    if (w == 4 || w == 7) alive = wait_class(c) && alive;
    if (w == 7) alive = load_kk(0) && alive;
    if (w == 5 || w == 6) alive = wait_class(c) && alive;
    for (int s = 0; s < NSEG; ++s) {
      const int slot = s & 3;
      if (s > 0) {
        const int o0 = xch_off(slot, 0, c.cls) + (2 * u) * 1024 + lane * 16;
        v4u m0, m1;
        unsigned spins = 0; bool good;
        do {
          asm volatile("" ::: "memory");
          m0 = ld128s(c.rs, o0);
          if (u < 3) m1 = ld128s(c.rs, o0 + 1024); else m1 = m0;
          good = umax(mx4(m0), mx4(m1)) != SENT;
          if (++spins > SPIN_CAP) { cw_set(c, CW_TMO, 1u); alive = false; break; }
        } while (!good);
        *(v4u*)(lds + L_MEM + (2 * u) * 1024 + lane * 16) = m0;
        if (u < 3) *(v4u*)(lds + L_MEM + (2 * u + 1) * 1024 + lane * 16) = m1;
        alive = __all(alive);
      }
      lbar();
      if (w == 5 && s > 0 && lane == 0) cw_set(c, CW_PM + (h * 4 + g) * 16, (unsigned)s);
      if (w == 7 && s < NSEG - 1) {
        __builtin_amdgcn_s_setprio(2);
        v4u mf[7];
#pragma unroll
        for (int k = 0; k < 7; ++k) mf[k] = *(const v4u*)(lds + L_MEM + k * 1024 + lane * 16);
        f32x4 ax = {0, 0, 0, 0};
#pragma unroll
        for (int k = 0; k < 7; ++k) ax = mfma(kf[k], mf[k], ax);
        const bool val = (gq >> 1) == ((lane >> 3) & 1);
        float sx[4];
#pragma unroll
        for (int r = 0; r < 4; ++r) { float uu = val ? ax[r] : 0.f; uu += ror8(uu); sx[r] = swap32_add(uu); }
        float m = fmaxf(fmaxf(sx[0], sx[1]), fmaxf(sx[2], sx[3]));
        m = swap16_max(m);
        const int ro = xch_off(slot, recblk, c.cls) + recin;
        if (c16 < 8 && gq == 0) st32s(canon(__float_as_uint(m)), c.rs, ro + 384 + c16 * 4);
        const float mb = -m * LOG2E;
        float e[4];
#pragma unroll
        for (int r = 0; r < 4; ++r) e[r] = ex2(fmaf(sx[r], LOG2E, mb));
        if (c16 < 8 && gq < 2) st64s(v2u{canon(pk2(e[0], e[1])), canon(pk2(e[2], e[3]))}, c.rs, ro + 256 + c16 * 16 + gq * 8);
        __builtin_amdgcn_s_setprio(0);
      }
      lbar();
      if (w == 7 && s + 1 < NSEG - 1) alive = alive && load_kk(s + 1);
      if (w == 4 && s < NSEG - 1) {
        const int i = lane & 7, gg = (lane >> 3) & 1;
        f32x4 t0 = *(const f32x4*)(lds + L_P + ((0 + gg) * 8 + i) * 16), t1 = *(const f32x4*)(lds + L_P + ((2 + gg) * 8 + i) * 16);
        f32x4 t2 = *(const f32x4*)(lds + L_P + ((4 + gg) * 8 + i) * 16), t3 = *(const f32x4*)(lds + L_P + ((6 + gg) * 8 + i) * 16);
        f32x4 o = (t0 + t1) + (t2 + t3);
        const int ro = xch_off(slot, recblk, c.cls) + recin;
        if (lane < 16) st64s(v2u{canon(pk2(o[0], o[1])), canon(pk2(o[2], o[3]))}, c.rs, ro + i * 16 + gg * 8);
        if (lane < 26) st128s(v4u{SENT, SENT, SENT, SENT}, c.rs, xch_off((s + 2) & 3, recblk, c.cls) + recin + lane * 16);
      }
      if (!alive) break;
    }
    lbar();
    if (w == 5 && lane == 0) {
      if (alive) cw_set(c, CW_PM + (h * 4 + g) * 16, (unsigned)NSEG);
      if (h == 0 && g == 0) __hip_atomic_store(c.cw + CW_EPOCH, c.tagw >> 8, RLX_AGENT);
    }
  }
}

DI void role_O(const Params& p, Ctx& c, unsigned char* lds, int oc) {
  const int tid = threadIdx.x, lane = tid & 63, w = __builtin_amdgcn_readfirstlane(tid >> 6);
  const int gq = lane >> 4, c16 = lane & 15;
  bool alive = wait_vec(c, CW_PF + (16 + 2 * w + (lane & 1)) * 8 + 2 * (oc / 7) + ((lane >> 1) & 1), 1u, 1);
  v4u wb[2][14];
#pragma unroll
  for (int hh = 0; hh < 2; ++hh)
#pragma unroll
    for (int ks = 0; ks < 14; ++ks) wb[hh][ks] = ld_wf(c, 448 + (2 * w + hh) * 28 + oc, ks, lane);
  if (w < 7) *(v4u*)(lds + L_MEM + w * 1024 + lane * 16) = ld_mem0(p, w, lane);
  const int half = oc >= 14 ? 1 : 0;
  unsigned vprog = 0;
  v4u vx[2];
  auto load_vv = [&](int s) -> bool {
    if (vprog < (unsigned)s + 1) {
      if (!wait_vec(c, CW_PROG + (32 + 2 * (2 * w + (lane & 1)) + half) * 16, s + 1, 1)) return false;
      vprog = s + 1;
    }
#pragma unroll
    for (int hh = 0; hh < 2; ++hh) vx[hh] = ld128s(c.rs, (int)(VVF_OFF + ((size_t)((s * 28 + oc) * 16 + 2 * w + hh) * 64 + lane) * 16));
    return true;
  };
  vdrain();
  alive = wait_all_prep(c, lane) && alive;
  alive = wait_class(c) && alive;
  alive = load_vv(0) && alive;
  const int rh = 2 * w + ((lane >> 3) & 1), ri = lane & 7;
  const int rec = (rh * 4 + gq) * 512, recblk = 1 + (rec >> 13), recin = rec & 8191;
  const int oks = oc >> 1, oksq = oks >= 7 ? oks - 7 : oks, ohf = oks >= 7 ? 1 : 0;
  v4u sel;
#pragma unroll
  for (int k = 0; k < 4; ++k) sel[k] = (c16 & 7) == 2 * k ? 0x00003F80u : ((c16 & 7) == 2 * k + 1 ? 0x3F800000u : 0u);
  const bool diag = (gq >> 1) == (c16 >> 3);
  for (int s = 0; s < NSEG; ++s) {
    const int slot = s & 3;
    if (w < 7) {
      if (s > 0) {
        v4u m;
        alive = alive && poll128(c, xch_off(slot, 0, c.cls) + w * 1024 + lane * 16, m);
        *(v4u*)(lds + L_MEM + w * 1024 + lane * 16) = m;
      }
    } else vdrain();
    lbar();
    if (w == 7 && s > 0 && lane == 0) cw_set(c, CW_PM + (64 + oc) * 16, (unsigned)s);
    v4u mf[7];
#pragma unroll
    for (int k = 0; k < 7; ++k) mf[k] = *(const v4u*)(lds + L_MEM + k * 1024 + lane * 16);
    v2u zz;
    {
      float z[2][4];
#pragma unroll
      for (int hh = 0; hh < 2; ++hh) {
        f32x4 aA = {0, 0, 0, 0}, aB = {0, 0, 0, 0};
#pragma unroll
        for (int k = 0; k < 7; ++k) { aA = mfma(mf[k], wb[hh][k], aA); aB = mfma(mf[k], wb[hh][k + 7], aB); }
#pragma unroll
        for (int r = 0; r < 4; ++r) z[hh][r] = swap32_add(gq < 2 ? aA[r] : aB[r]);
      }
      zz = gq < 2 ? v2u{pk2(z[0][0], z[0][1]), pk2(z[0][2], z[0][3])} : v2u{pk2(z[1][0], z[1][1]), pk2(z[1][2], z[1][3])};
    }
    asm volatile("" :: "v"(zz));
    if (s < NSEG - 1) {
      const int ro = xch_off(slot, recblk, c.cls) + recin;
      v4u c1, c2; unsigned c3;
      for (unsigned spins = 0;; ++spins) {
        asm volatile("" ::: "memory");
        c3 = ld32s(c.rs, ro + 384 + ri * 4); c2 = ld128s(c.rs, ro + 256 + ri * 16); c1 = ld128s(c.rs, ro + ri * 16);
        if (__all(umax(mx4(c2), c3) != SENT)) break;
        if (spins > SPIN_CAP) { cw_set(c, CW_TMO, 1u); alive = false; break; }
      }
      if (!__all(ok4(c1))) { asm volatile("" ::: "memory"); c1 = ld128s(c.rs, ro + ri * 16); }
      const float mg = __uint_as_float(c3);
      const float mx = swap32_max(swap16_max(mg));
      const float f = ex2((mg - mx) * LOG2E);
      float ef[8];
#pragma unroll
      for (int k = 0; k < 4; ++k) { ef[2 * k] = bflo(c2[k]); ef[2 * k + 1] = bfhi(c2[k]); }
      const float lg = ((ef[0] + ef[1]) + (ef[2] + ef[3])) + ((ef[4] + ef[5]) + (ef[6] + ef[7]));
      const float lx = swap32_add(swap16_add(f * lg));
      v4u ax;
#pragma unroll
      for (int k = 0; k < 4; ++k) ax[k] = pk2(ef[2 * k] * f, ef[2 * k + 1] * f);
      const f32x4 oa = mfma(vx[0], ax, f32x4{0, 0, 0, 0}), ob = mfma(vx[1], ax, f32x4{0, 0, 0, 0});
      f32x4 ox;
#pragma unroll
      for (int r = 0; r < 4; ++r) ox[r] = c16 < 8 ? oa[r] : ob[r];
      const float mxl = mx * LOG2E;
      asm volatile("" :: "v"(ox), "v"(lx), "v"(mxl));
      for (unsigned spins = 0; !__all(ok4(c1)); ++spins) {
        asm volatile("" ::: "memory");
        c1 = ld128s(c.rs, ro + ri * 16);
        if (spins > SPIN_CAP) { cw_set(c, CW_TMO, 1u); alive = false; break; }
      }
      const f32x4 sc = mfma(sel, c1, f32x4{0, 0, 0, 0});
      const float ml = swap16_max(fmaxf(fmaxf(sc[0], sc[1]), fmaxf(sc[2], sc[3]))) * LOG2E;
      const float mb = -fmaxf(ml, mxl);
      float e[4];
#pragma unroll
      for (int r = 0; r < 4; ++r) e[r] = ex2(fmaf(sc[r], LOG2E, mb));
      const float cx = ex2(mxl + mb);
      const float l = fmaf(cx, lx, swap16_add((e[0] + e[1]) + (e[2] + e[3])));
      const float rl = __builtin_amdgcn_rcpf(l);
      const float rm = diag ? rl : 0.f, kx = cx * rl;
      f32x4 o = mfma(v4u{zz.x, zz.y, 0u, 0u}, v4u{pk2(e[0] * rm, e[1] * rm), pk2(e[2] * rm, e[3] * rm), 0u, 0u}, ox * kx);
#pragma unroll
      for (int r = 0; r < 4; ++r) o[r] += ror8(o[r]);
      if (c16 < 8) *(f32x4*)(lds + L_C + ((w * 8 + c16) * 16 + 4 * gq) * 4) = o;
      lbar();
      if (w == 7) {
        const int i = (lane >> 2) & 7, mp = (lane >> 5) * 4 + (lane & 3);
        f32x2 t[8];
#pragma unroll
        for (int k = 0; k < 8; ++k) t[k] = *(const f32x2*)(lds + L_C + ((k * 8 + i) * 16 + 2 * mp) * 4);
        f32x2 sum = ((t[0] + t[1]) + (t[2] + t[3])) + ((t[4] + t[5]) + (t[6] + t[7]));
        const int pos = oksq * 1024 + ((2 * (oc & 1) + (lane >> 5)) * 16 + ohf * 8 + i) * 16 + (lane & 3) * 4;
        st32s(canon(pk2(sum[0], sum[1])), c.rs, xch_off((s + 1) & 3, 0, c.cls) + pos);
        st32s(SENT, c.rs, xch_off((s + 3) & 3, 0, c.cls) + pos);
      }
    }
    st64s(zz, c.rs, (int)(VMF_OFF + ((size_t)((s * 28 + oc) * 8 + w) * 64 + lane) * 8));
    if (s + 1 < NSEG - 1) alive = alive && load_vv(s + 1);
    if (!alive) break;
  }
  vdrain();
  lbar();
  if (w == 7 && alive && lane == 0) cw_set(c, CW_PM + (64 + oc) * 16, (unsigned)NSEG);
}

constexpr int XRS = 912, XB = 32 * XRS;
DI int xrow_addr(int q) { return (q / 56) * XRS + (q % 56) * 16; }
DI void role_KV(const Params& p, Ctx& c, unsigned char* lds, int hm2) {
  const int tid = threadIdx.x, lane = tid & 63, w = __builtin_amdgcn_readfirstlane(tid >> 6);
  const int gq = lane >> 4, c16 = lane & 15;
  const int m = hm2 >> 1, half = hm2 & 1;
  const bool iskk = m < 16;
  const int h = m & 15;
  constexpr int SPB = 28 * 1024, L_SP = 2 * XB;
  constexpr int EARLY = 2;
  if (w == 7) {
    auto reduce = [&](int s) {
      const unsigned char* sp = lds + L_SP + (s & 1) * SPB + lane * 16;
#pragma unroll
      for (int qt = 0; qt < 2; ++qt) {
        f32x4 a0 = *(const f32x4*)(sp + qt * 1024), a1 = *(const f32x4*)(sp + (2 + qt) * 1024);
#pragma unroll
        for (int w2 = 1; w2 < 7; ++w2) { a0 += *(const f32x4*)(sp + (w2 * 4 + qt) * 1024); a1 += *(const f32x4*)(sp + (w2 * 4 + 2 + qt) * 1024); }
        st128s(v4u{pk2(a0[0], a0[1]), pk2(a0[2], a0[3]), pk2(a1[0], a1[1]), pk2(a1[2], a1[3])}, c.rs, (int)(SXX_OFF + ((size_t)(((s * 16 + h) * 2 + half) * 2 + qt) * 64 + lane) * 16));
      }
    };
    const u16* xs = p.x + lane * 8;
    v4u t[28];
#pragma unroll
    for (int f = 0; f < 28; ++f) t[f] = *(const v4u*)(xs + f * 512);
#pragma unroll
    for (int f = 0; f < 28; ++f) *(v4u*)(lds + xrow_addr(f * 64 + lane)) = t[f];
    for (int s = 0; s < NSEG; ++s) {
      lbar();
      const int sn = s + 1 < NSEG ? s + 1 : s;
#pragma unroll
      for (int f = 0; f < 28; ++f) t[f] = *(const v4u*)(xs + (size_t)sn * (32 * D) + f * 512);
      if (iskk && s > 0) reduce(s - 1);
#pragma unroll
      for (int f = 0; f < 28; ++f) *(v4u*)(lds + ((s + 1) & 1) * XB + xrow_addr(f * 64 + lane)) = t[f];
      vdrain();
      lbar();
      if (lane == 0 && s != EARLY) cw_set(c, CW_PROG + hm2 * 16, (unsigned)(s < EARLY ? s + 1 : s));
    }
    lbar();
    if (lane == 0) cw_set(c, CW_PROG + hm2 * 16, (unsigned)NSEG);
    if (iskk) {
      reduce(NSEG - 1);
      vdrain();
      if (lane == 0) cw_set(c, CW_PROG + hm2 * 16, (unsigned)(NSEG + 2));
    }
    return;
  }
  wait_vec(c, CW_PF + m * 8 + 4 * half + (lane & 3), 1u, 1);
  v4u wt[2][14];
#pragma unroll
  for (int t2 = 0; t2 < 2; ++t2)
#pragma unroll
    for (int ks = 0; ks < 14; ++ks) wt[t2][ks] = ld_wf(c, m * 28 + 14 * half + 2 * w + t2, ks, lane);
  vdrain();
  for (int s = 0; s < NSEG; ++s) {
    lbar();
    const unsigned char* xb = lds + (s & 1) * XB;
    const unsigned char* xf0 = xb + c16 * XRS + gq * 16;
    f32x4 acc[2][2];
#pragma unroll
    for (int a = 0; a < 2; ++a) { acc[a][0] = f32x4{0, 0, 0, 0}; acc[a][1] = f32x4{0, 0, 0, 0}; }
    if (iskk) {
#pragma unroll
      for (int ks = 0; ks < 14; ++ks)
#pragma unroll
        for (int rt = 0; rt < 2; ++rt) {
          v4u xf = *(const v4u*)(xf0 + rt * 16 * XRS + ks * 64);
          acc[rt][0] = mfma(wt[0][ks], xf, acc[rt][0]); acc[rt][1] = mfma(wt[1][ks], xf, acc[rt][1]);
        }
      const int gq2 = (gq >> 1) + 2 * (gq & 1);
      v4u kf[2];
#pragma unroll
      for (int rt = 0; rt < 2; ++rt) {
        unsigned x0 = pk2(acc[rt][0][0], acc[rt][0][1]), x1 = pk2(acc[rt][0][2], acc[rt][0][3]);
        unsigned y0 = pk2(acc[rt][1][0], acc[rt][1][1]), y1 = pk2(acc[rt][1][2], acc[rt][1][3]);
        auto s0 = __builtin_amdgcn_permlane16_swap(x0, y0, false, false);
        auto s1 = __builtin_amdgcn_permlane16_swap(x1, y1, false, false);
        kf[rt] = v4u{s0[0], s1[0], s0[1], s1[1]};
      }
      vdrain();
#pragma unroll
      for (int rt = 0; rt < 2; ++rt) {
        const int g = c16 >> 2, rowq = (c16 & 3) + 4 * rt;
        st128s(kf[rt], c.rs, (int)(KKF_OFF + ((size_t)(((s * 16 + h) * 4 + g) * 7 + w) * 64 + rowq + 8 * half + 16 * gq2) * 16));
      }
#pragma unroll
      for (int qt = 0; qt < 2; ++qt) {
        const v4u xq = *(const v4u*)(xb + (16 * qt + c16) * XRS + (7 * half + w) * 64 + gq2 * 16);
#pragma unroll
        for (int kt = 0; kt < 2; ++kt)
          *(f32x4*)(lds + L_SP + (s & 1) * SPB + (w * 4 + kt * 2 + qt) * 1024 + lane * 16) = mfma(kf[kt], xq, f32x4{0, 0, 0, 0});
      }
    } else {
#pragma unroll
      for (int ks = 0; ks < 14; ++ks)
#pragma unroll
        for (int rt = 0; rt < 2; ++rt) {
          v4u xf = *(const v4u*)(xf0 + rt * 16 * XRS + ks * 64);
          acc[rt][0] = mfma(xf, wt[0][ks], acc[rt][0]); acc[rt][1] = mfma(xf, wt[1][ks], acc[rt][1]);
        }
      v4u o[2];
#pragma unroll
      for (int t2 = 0; t2 < 2; ++t2) o[t2] = v4u{pk2(acc[0][t2][0], acc[0][t2][1]), pk2(acc[0][t2][2], acc[0][t2][3]), pk2(acc[1][t2][0], acc[1][t2][1]), pk2(acc[1][t2][2], acc[1][t2][3])};
      vdrain();
#pragma unroll
      for (int t2 = 0; t2 < 2; ++t2) st128s(o[t2], c.rs, (int)(VVF_OFF + ((size_t)((s * 28 + 14 * half + 2 * w + t2) * 16 + h) * 64 + lane) * 16));
    }
    if (s < EARLY) vdrain();
    lbar();
  }
  vdrain();
  lbar();
}

constexpr int LT_X = 0, LT_PX = 30 * 1024, LT_PM = 62 * 1024;
DI void task_T(const Params& p, const Ctx& c, unsigned char* lds, int seg, int cg) {
  const int tid = threadIdx.x, lane = tid & 63, w = __builtin_amdgcn_readfirstlane(tid >> 6);
  const int gq = lane >> 4, c16 = lane & 15;
  const int oc = 7 * cg + (w < 7 ? w : 0);
#pragma unroll
  for (int i = 0; i < 4; ++i) {
    const int q = tid + 512 * i;
    if (q < 1792) *(v4u*)(lds + LT_X + xrow_addr(q)) = *(const v4u*)(p.x + (size_t)seg * (32 * D) + q * 8);
  }
  if (w == 7) wait_vec(c, CW_PROG + lane * 16, (unsigned)seg + (lane < 32 ? 2u : 1u), 4);
  lbar();
  v4u sx[2][2][2], vv[16];
#pragma unroll
  for (int hh = 0; hh < 2; ++hh)
#pragma unroll
    for (int hf = 0; hf < 2; ++hf)
#pragma unroll
      for (int qt = 0; qt < 2; ++qt) sx[hh][hf][qt] = ld128s(c.rs, (int)(SXX_OFF + ((size_t)(((seg * 16 + 2 * w + hh) * 2 + hf) * 2 + qt) * 64 + lane) * 16));
  if (w < 7) {
#pragma unroll
    for (int h = 0; h < 16; ++h) vv[h] = ld128s(c.rs, (int)(VVF_OFF + ((size_t)((seg * 28 + oc) * 16 + h) * 64 + lane) * 16));
  } else {
    wait_vec(c, CW_PM + (cg * 16 + (seg & 15)) * 16, seg + 1, 4);
    wait_vec(c, CW_PM + lane * 16, seg + 1, 1);
    wait_vec(c, CW_PM + (64 + (lane < 28 ? lane : 0)) * 16, seg + 1, 1);
  }
  lbar();
  v4u kf[14]; v2u vm[8];
#pragma unroll
  for (int ks = 0; ks < 14; ++ks) kf[ks] = ld128s(c.rs, (int)(KKM_OFF + (size_t)((seg * 16 + 2 * w + (c16 >> 3)) * 56 + 4 * ks + gq) * 128 + (c16 & 7) * 16));
  if (w < 7) {
#pragma unroll
    for (int pr = 0; pr < 8; ++pr) vm[pr] = ld64s(c.rs, (int)(VMF_OFF + ((size_t)((seg * 28 + oc) * 8 + pr) * 64 + lane) * 8));
  }
#pragma unroll
  for (int qt = 0; qt < 2; ++qt) {
    const unsigned char* xq = lds + LT_X + (16 * qt + c16) * XRS + gq * 16;
    f32x4 am = {0, 0, 0, 0}, am2 = {0, 0, 0, 0};
#pragma unroll
    for (int ks = 0; ks < 14; ks += 2) {
      am = mfma(kf[ks], *(const v4u*)(xq + ks * 64), am);
      am2 = mfma(kf[ks + 1], *(const v4u*)(xq + ks * 64 + 64), am2);
    }
    am += am2;
    const int q = 16 * qt + c16;
    float sc[2][8], mxh[2];
#pragma unroll
    for (int hh = 0; hh < 2; ++hh) {
      float m = (gq >> 1) == hh ? fmaxf(fmaxf(am[0], am[1]), fmaxf(am[2], am[3])) : -1e9f;
#pragma unroll
      for (int k = 0; k < 4; ++k) {
        const unsigned u0 = sx[hh][0][qt][k], u1 = sx[hh][1][qt][k];
        const int key = (k < 2 ? 4 * gq + 2 * k : 16 + 4 * gq + 2 * (k - 2));
        sc[hh][2 * k] = key <= q ? bflo(u0) + bflo(u1) : -1e9f;
        sc[hh][2 * k + 1] = key + 1 <= q ? bfhi(u0) + bfhi(u1) : -1e9f;
        m = fmaxf(m, fmaxf(sc[hh][2 * k], sc[hh][2 * k + 1]));
      }
      mxh[hh] = swap32_max(swap16_max(m)) * LOG2E;
    }
    float em[4], lh[2];
    const float mm = gq < 2 ? mxh[0] : mxh[1];
#pragma unroll
    for (int r = 0; r < 4; ++r) em[r] = ex2(fmaf(am[r], LOG2E, -mm));
    const float lm = (em[0] + em[1]) + (em[2] + em[3]);
#pragma unroll
    for (int hh = 0; hh < 2; ++hh) {
      float l = (gq >> 1) == hh ? lm : 0.f;
#pragma unroll
      for (int k = 0; k < 8; ++k) { sc[hh][k] = ex2(fmaf(sc[hh][k], LOG2E, -mxh[hh])); l += sc[hh][k]; }
      lh[hh] = __builtin_amdgcn_rcpf(swap32_add(swap16_add(l)));
      *(v4u*)(lds + LT_PX + ((2 * w + hh) * 2 + qt) * 1024 + lane * 16) =
          v4u{pk2(sc[hh][0] * lh[hh], sc[hh][1] * lh[hh]), pk2(sc[hh][2] * lh[hh], sc[hh][3] * lh[hh]), pk2(sc[hh][4] * lh[hh], sc[hh][5] * lh[hh]), pk2(sc[hh][6] * lh[hh], sc[hh][7] * lh[hh])};
    }
    const float rm = gq < 2 ? lh[0] : lh[1];
    *(v2u*)(lds + LT_PM + (w * 2 + qt) * 512 + lane * 8) = v2u{pk2(em[0] * rm, em[1] * rm), pk2(em[2] * rm, em[3] * rm)};
  }
  lbar();
  if (w < 7) {
    f32x4 o[2] = {f32x4{0, 0, 0, 0}, f32x4{0, 0, 0, 0}};
#pragma unroll
    for (int h = 0; h < 16; ++h)
#pragma unroll
      for (int qt = 0; qt < 2; ++qt) o[qt] = mfma(vv[h], *(const v4u*)(lds + LT_PX + (h * 2 + qt) * 1024 + lane * 16), o[qt]);
#pragma unroll
    for (int i = 0; i < 4; ++i)
#pragma unroll
      for (int qt = 0; qt < 2; ++qt) {
        const v2u b0 = *(const v2u*)(lds + LT_PM + ((2 * i) * 2 + qt) * 512 + lane * 8), b1 = *(const v2u*)(lds + LT_PM + ((2 * i + 1) * 2 + qt) * 512 + lane * 8);
        o[qt] = mfma(v4u{vm[2 * i].x, vm[2 * i].y, vm[2 * i + 1].x, vm[2 * i + 1].y}, v4u{b0.x, b0.y, b1.x, b1.y}, o[qt]);
      }
#pragma unroll
    for (int qt = 0; qt < 2; ++qt)
      *(v2u*)(p.out + (size_t)(32 * seg + 16 * qt + c16) * (2 * D) + 16 * oc + 4 * gq) = v2u{pk2(o[qt][0], o[qt][1]), pk2(o[qt][2], o[qt][3])};
  }
  lbar();
}

DI unsigned phi2(float a, float b) {
  const unsigned t = pk2(a, b);
  const float x = bflo(t), y = bfhi(t);
  const unsigned u = pk2(x > 0.f ? x + 1.f : __expf(x), y > 0.f ? y + 1.f : __expf(y));
  return pk2(bflo(u) + 1e-6f, bfhi(u) + 1e-6f);
}
constexpr int LB_W = 0, LB_R = 448 * 192;
DI v4u wfrag(const unsigned char* lds, int off) {
  typedef short v4s __attribute__((ext_vector_type(4)));
  v4s lo = __builtin_amdgcn_ds_read_tr16_b64_v4i16((LAS v4s*)((LAS unsigned char*)lds + off));
  v4s hi = __builtin_amdgcn_ds_read_tr16_b64_v4i16((LAS v4s*)((LAS unsigned char*)lds + off + 4 * 192));
  v2u l2 = __builtin_bit_cast(v2u, lo), h2 = __builtin_bit_cast(v2u, hi);
  return v4u{l2.x, l2.y, h2.x, h2.y};
}
DI void task_B1(const Params& p, const Ctx& c, unsigned char* lds, int h, int tq) {
  const int tid = threadIdx.x, lane = tid & 63, w = __builtin_amdgcn_readfirstlane(tid >> 6);
  const int gq = lane >> 4, c16 = lane & 15;
#pragma unroll 7
  for (int i = 0; i < 21; ++i) {
    const int idx = tid + 512 * i, k = idx / 24, rem = idx - k * 24, m = rem >> 3, sl = rem & 7;
    v2u v = {0u, 0u};
    if (sl < 7) v = *(const v2u*)(p.wqkv + (size_t)k * (3 * D) + m * D + h * 28 + sl * 4);
    *(v2u*)(lds + LB_W + k * 192 + m * 64 + sl * 8) = v;
  }
  if (w < 7) {
    const int nt = 7 * tq + w;
    unsigned e8[8];
#pragma unroll
    for (int i = 0; i < 8; ++i) { const int e = (i < 4 ? 4 * gq + i : 12 + 4 * gq + i); e8[i] = e < 28 ? (unsigned)p.wo[(size_t)(28 * h + e) * D + 16 * nt + c16] : 0u; }
    st128s(v4u{e8[0] | e8[1] << 16, e8[2] | e8[3] << 16, e8[4] | e8[5] << 16, e8[6] | e8[7] << 16}, c.rs, (int)(WOF_OFF + ((size_t)(h * 28 + nt) * 64 + lane) * 16));
  }
  lbar();
  const int t0 = 512 * tq + 64 * w;
  const u16* xr = p.bx + (size_t)(t0 + c16) * D + 8 * gq;
  const int wb = LB_W + (8 * gq + (c16 >> 2)) * 192 + (lane & 3) * 8;
  f32x4 aq[4][2], ak[4][2], av[4][2];
#pragma unroll
  for (int rt = 0; rt < 4; ++rt)
#pragma unroll
    for (int dt = 0; dt < 2; ++dt) { aq[rt][dt] = f32x4{0, 0, 0, 0}; ak[rt][dt] = f32x4{0, 0, 0, 0}; av[rt][dt] = f32x4{0, 0, 0, 0}; }
  v4u xn[4];
#pragma unroll
  for (int rt = 0; rt < 4; ++rt) xn[rt] = *(const v4u*)(xr + (size_t)(16 * rt) * D);
#pragma unroll 1
  for (int ks = 0; ks < 14; ++ks) {
    v4u x[4];
#pragma unroll
    for (int rt = 0; rt < 4; ++rt) x[rt] = xn[rt];
    const int kn = ks + 1 < 14 ? ks + 1 : ks;
#pragma unroll
    for (int rt = 0; rt < 4; ++rt) xn[rt] = *(const v4u*)(xr + (size_t)(16 * rt) * D + 32 * kn);
#pragma unroll
    for (int dt = 0; dt < 2; ++dt) {
      const v4u wq = wfrag(lds, wb + ks * 32 * 192 + dt * 32), wk = wfrag(lds, wb + ks * 32 * 192 + 64 + dt * 32), wv = wfrag(lds, wb + ks * 32 * 192 + 128 + dt * 32);
#pragma unroll
      for (int rt = 0; rt < 4; ++rt) { aq[rt][dt] = mfma(wq, x[rt], aq[rt][dt]); ak[rt][dt] = mfma(x[rt], wk, ak[rt][dt]); av[rt][dt] = mfma(x[rt], wv, av[rt][dt]); }
    }
  }
#pragma unroll
  for (int rt = 0; rt < 4; ++rt)
    st128s(v4u{phi2(aq[rt][0][0], aq[rt][0][1]), phi2(aq[rt][0][2], aq[rt][0][3]), phi2(aq[rt][1][0], aq[rt][1][1]), phi2(aq[rt][1][2], aq[rt][1][3])}, c.rs,
           (int)(PQ_OFF + ((size_t)((t0 / 16 + rt) * 16 + h) * 64 + lane) * 16));
  f32x4 kv[2][2] = {{f32x4{0, 0, 0, 0}, f32x4{0, 0, 0, 0}}, {f32x4{0, 0, 0, 0}, f32x4{0, 0, 0, 0}}};
#pragma unroll
  for (int rp = 0; rp < 2; ++rp) {
    v4u pk[2], vf[2];
#pragma unroll
    for (int dt = 0; dt < 2; ++dt) {
      const f32x4 k0 = ak[2 * rp][dt], k1 = ak[2 * rp + 1][dt], v0 = av[2 * rp][dt], v1 = av[2 * rp + 1][dt];
      pk[dt] = v4u{phi2(k0[0], k0[1]), phi2(k0[2], k0[3]), phi2(k1[0], k1[1]), phi2(k1[2], k1[3])};
      vf[dt] = v4u{pk2(v0[0], v0[1]), pk2(v0[2], v0[3]), pk2(v1[0], v1[1]), pk2(v1[2], v1[3])};
    }
    if (c16 >= 12) pk[1] = v4u{0u, 0u, 0u, 0u};
    if (c16 == 12) vf[1] = v4u{0x3F803F80u, 0x3F803F80u, 0x3F803F80u, 0x3F803F80u};
#pragma unroll
    for (int dt = 0; dt < 2; ++dt)
#pragma unroll
      for (int et = 0; et < 2; ++et) kv[dt][et] = mfma(pk[dt], vf[et], kv[dt][et]);
  }
#pragma unroll
  for (int t = 0; t < 4; ++t) *(f32x4*)(lds + LB_R + (w * 4 + t) * 1024 + lane * 16) = kv[t >> 1][t & 1];
  lbar();
  if (w < 4) {
    f32x4 a = *(const f32x4*)(lds + LB_R + w * 1024 + lane * 16);
#pragma unroll
    for (int w2 = 1; w2 < 8; ++w2) a += *(const f32x4*)(lds + LB_R + (w2 * 4 + w) * 1024 + lane * 16);
    st128s(__builtin_bit_cast(v4u, a), c.rs, (int)(KVP_OFF + ((size_t)((h * 4 + tq) * 4 + w) * 64 + lane) * 16));
  }
  vdrain();
  lbar();
  if (tid == 0) cw_set(c, CW_B1 + h * 4 + tq, 1u);
}

constexpr int LB_OF = 0;
DI void task_B2(const Params& p, const Ctx& c, unsigned char* lds, int tt) {
  const int tid = threadIdx.x, lane = tid & 63, w = __builtin_amdgcn_readfirstlane(tid >> 6);
  const int gq = lane >> 4;
  const int c16 = lane & 15;
  if (w == 0) wait_vec(c, CW_B1 + lane, 1u, 4);
  lbar();
  v4u pq[2][4];
#pragma unroll
  for (int hh = 0; hh < 2; ++hh)
#pragma unroll
    for (int t = 0; t < 4; ++t) pq[hh][t] = ld128s(c.rs, (int)(PQ_OFF + ((size_t)((4 * tt + t) * 16 + 2 * w + hh) * 64 + lane) * 16));
  v4u kva[2][2];
#pragma unroll
  for (int hh = 0; hh < 2; ++hh)
#pragma unroll
    for (int et = 0; et < 2; ++et) {
      f32x4 d0 = {0, 0, 0, 0}, d1 = {0, 0, 0, 0};
#pragma unroll
      for (int tq = 0; tq < 4; ++tq) {
        d0 += __builtin_bit_cast(f32x4, ld128s(c.rs, (int)(KVP_OFF + ((size_t)(((2 * w + hh) * 4 + tq) * 4 + et) * 64 + lane) * 16)));
        d1 += __builtin_bit_cast(f32x4, ld128s(c.rs, (int)(KVP_OFF + ((size_t)(((2 * w + hh) * 4 + tq) * 4 + 2 + et) * 64 + lane) * 16)));
      }
      kva[hh][et] = v4u{pk2(d0[0], d0[1]), pk2(d0[2], d0[3]), pk2(d1[0], d1[1]), pk2(d1[2], d1[3])};
    }
#pragma unroll
  for (int hh = 0; hh < 2; ++hh)
#pragma unroll
    for (int t = 0; t < 4; ++t) {
      const f32x4 n0 = mfma(kva[hh][0], pq[hh][t], f32x4{0, 0, 0, 0}), n1 = mfma(kva[hh][1], pq[hh][t], f32x4{0, 0, 0, 0});
      float den = swap32_add(swap16_add(gq == 3 ? n1[0] : 0.f));
      den = bflo(pk2(bflo(pk2(den, 0.f)) + 1e-6f, 0.f));
      const float rd = 1.f / den;
      const unsigned a0 = pk2(n0[0], n0[1]), a1 = pk2(n0[2], n0[3]), b0 = pk2(n1[0], n1[1]), b1 = pk2(n1[2], n1[3]);
      v4u o = {pk2(bflo(a0) * rd, bfhi(a0) * rd), pk2(bflo(a1) * rd, bfhi(a1) * rd), pk2(bflo(b0) * rd, bfhi(b0) * rd), pk2(bflo(b1) * rd, bfhi(b1) * rd)};
      if (gq == 3) { o.z = 0u; o.w = 0u; }
      *(v4u*)(lds + LB_OF + ((2 * w + hh) * 4 + t) * 1024 + lane * 16) = o;
    }
  lbar();
  f32x4 acc[4][4];
#pragma unroll
  for (int j = 0; j < 4; ++j)
#pragma unroll
    for (int t = 0; t < 4; ++t) acc[j][t] = f32x4{0, 0, 0, 0};
  const int nj = w < 4 ? 4 : 3;
#pragma unroll 2
  for (int h = 0; h < 16; ++h) {
    v4u a[4], b[4];
#pragma unroll
    for (int j = 0; j < 4; ++j) a[j] = ld128s(c.rs, (int)(WOF_OFF + ((size_t)(h * 28 + (j < nj ? w + 8 * j : w)) * 64 + lane) * 16));
#pragma unroll
    for (int t = 0; t < 4; ++t) b[t] = *(const v4u*)(lds + LB_OF + (h * 4 + t) * 1024 + lane * 16);
#pragma unroll
    for (int j = 0; j < 4; ++j)
#pragma unroll
      for (int t = 0; t < 4; ++t) acc[j][t] = mfma(a[j], b[t], acc[j][t]);
  }
#pragma unroll
  for (int j = 0; j < 4; ++j)
    if (j < nj) {
#pragma unroll
      for (int t = 0; t < 4; ++t)
        *(v2u*)(p.out + (size_t)(64 * tt + 16 * t + c16) * (2 * D) + D + 16 * (w + 8 * j) + 4 * gq) = v2u{pk2(acc[j][t][0], acc[j][t][1]), pk2(acc[j][t][2], acc[j][t][3])};
    }
  lbar();
}

DI void role_W(const Params& p, const Ctx& c, unsigned char* lds, int wid) {
  task_B1(p, c, lds, wid >> 2, wid & 3);
  if ((wid & 1) == 0) task_B2(p, c, lds, wid >> 1);
#pragma unroll 1
  for (int k = 0; k < 4; ++k) { task_T(p, c, lds, 16 * k + (wid >> 2), wid & 3); }
}

__global__ void __launch_bounds__(512, 2) k_main(Params p) {
  extern __shared__ __attribute__((aligned(16))) unsigned char lds[];
  const int b = blockIdx.x, label = b & 7, rank = b >> 3;
  const int tid = threadIdx.x, lane = tid & 63, w = __builtin_amdgcn_readfirstlane(tid >> 6);
  Ctx c;
  c.rs = __builtin_amdgcn_make_buffer_rsrc(p.ws, 0, (int)WS_USED, 0x00020000);
  c.cw = (GAS u64*)(p.ws + CTRL_OFF);
  c.cls = 0;
  const u64 old = __hip_atomic_load(c.cw + CW_EPOCH, RLX_AGENT);
  const bool isS = label < 2, isO = (label == 2 || label == 3) && rank < 14;
  const bool docal = label == 3 && rank == 31;
  unsigned best[2] = {1u << 30, 1u << 30};
  if (w == 7) {
    if (docal) { if (lane < 12) st32s((unsigned)lane, c.rs, (int)XCH_OFF + (40 + lane) * 8192); }
    if (isS) {
      const int id = rank * 2 + label, rec = id * 512;
#pragma unroll
      for (int k = 0; k < 4; ++k)
        if (lane < 26) st128s(v4u{SENT, SENT, SENT, SENT}, c.rs, xch_off(k >> 1, 1 + (rec >> 13), k & 1) + (rec & 8191) + lane * 16);
    } else if (isO) {
      const int oc = rank * 2 + (label - 2), oks = oc >> 1, oksq = oks >= 7 ? oks - 7 : oks, ohf = oks >= 7 ? 1 : 0;
      const int pos = oksq * 1024 + ((2 * (oc & 1) + (lane >> 5)) * 16 + ohf * 8 + ((lane >> 2) & 7)) * 16 + (lane & 3) * 4;
#pragma unroll
      for (int k = 0; k < 4; ++k) st32s(SENT, c.rs, xch_off(1 + (k >> 1), 0, k & 1) + pos);
    }
  }
  {
    const int m = label * 4 + (rank >> 3), blk = rank & 7;
    if (m < 16) prep_block<true>(p, c, lds, m, blk >> 1, blk & 1, docal, best);
    else prep_block<false>(p, c, lds, m, blk >> 1, blk & 1, docal, best);
    const unsigned lo = __builtin_amdgcn_readfirstlane((unsigned)old), hi = __builtin_amdgcn_readfirstlane((unsigned)(old >> 32));
    c.tagw = ((((u64)hi << 32) | lo) + 1) << 8;
    vdrain();
    lbar();
    if (tid == 0) cw_set(c, CW_PF + m * 8 + blk, 1u);
    if (docal && tid == 448) {
      cw_set(c, CW_CLASS, best[1] < best[0] ? 3u : 1u);
    }
  }
  if (isS) { const int id = rank * 2 + label; role_S(p, c, lds, id >> 2, id & 3); }
  else if (isO) role_O(p, c, lds, rank * 2 + (label - 2));
  else if (label < 4) {}
  else if (rank < 16) role_KV(p, c, lds, rank * 4 + (label - 4));
  else role_W(p, c, lds, (rank - 16) * 4 + (label - 4));
}

static unsigned char* align_ws(void* d_ws) { return (unsigned char*)(((uintptr_t)d_ws + (2u << 20) - 1) & ~(uintptr_t)((2u << 20) - 1)); }
static void launch_v15(void* const* d_in, void* d_out, void* d_ws, hipStream_t stream) {
  Params p;
  p.x = (const u16*)d_in[0]; p.mem0 = (const u16*)d_in[1]; p.Wq = (const u16*)d_in[2]; p.Wk = (const u16*)d_in[3]; p.Wv = (const u16*)d_in[4]; p.Wo = (const u16*)d_in[5];
  p.bx = (const u16*)d_in[7]; p.wqkv = (const u16*)d_in[8]; p.wo = (const u16*)d_in[9];
  p.out = (u16*)d_out; p.ws = align_ws(d_ws);
  (void)hipFuncSetAttribute((const void*)k_main, hipFuncAttributeMaxDynamicSharedMemorySize, 128 * 1024);
  k_main<<<256, 512, 128 * 1024, stream>>>(p);
}
extern "C" void kernel_launch(void* const* d_in, const int* in_sizes, int n_in, void* d_out, int out_size, void* d_ws, size_t ws_size,
                              hipStream_t stream) {
  (void)in_sizes; (void)n_in; (void)out_size; (void)ws_size;
  launch_v15(d_in, d_out, d_ws, stream);
}
```

```cpp
#include <hip/hip_runtime.h>
#include <hip/hip_bf16.h>
#include <stdint.h>

typedef unsigned short u16;
typedef unsigned long long u64;
typedef unsigned v4u __attribute__((ext_vector_type(4)));
typedef unsigned v2u __attribute__((ext_vector_type(2)));
typedef float f32x4 __attribute__((ext_vector_type(4)));
typedef float f32x2 __attribute__((ext_vector_type(2)));
typedef short bf16x8 __attribute__((ext_vector_type(8)));
typedef __bf16 bf16x2_t __attribute__((ext_vector_type(2)));
#define GAS __attribute__((address_space(1)))
#define LAS __attribute__((address_space(3)))
#define DI __device__ __forceinline__
#define RLX_AGENT __ATOMIC_RELAXED, __HIP_MEMORY_SCOPE_AGENT

constexpr int D = 448, H = 16, E = 256, NSEG = 64;
constexpr unsigned SENT = 0xFFFFFFFFu;
constexpr int AUX_SC1 = 16;
constexpr float LOG2E = 1.4426950408889634f;
constexpr unsigned SPIN_CAP = 1u << 20;
#define NAP_OM 7

constexpr size_t XCH_OFF = 0;
constexpr size_t CTRL_OFF = 1u << 20;
constexpr size_t WF_OFF = 2u << 20;
constexpr size_t KKF_OFF = 16u << 20;
constexpr size_t VVF_OFF = 46u << 20;
constexpr size_t KKM_OFF = 76u << 20;
constexpr size_t VMF_OFF = 84u << 20;
constexpr size_t SXX_OFF = 92u << 20;
constexpr size_t KVP_OFF = 97u << 20;
constexpr size_t WOF_OFF = 98u << 20;
constexpr size_t PQ_OFF = 100u << 20;
constexpr size_t WS_USED = 128u << 20;
constexpr int CW_EPOCH = 0;
constexpr int CW_CLASS = 16;
constexpr int CW_TMO = 32;
constexpr int CW_PF = 64;
constexpr int CW_B1 = CW_PF + 256;
constexpr int CW_PROG = CW_B1 + 64;
constexpr int CW_PM = CW_PROG + 64 * 16;

DI unsigned pk2(float a, float b) { f32x2 v = {a, b}; bf16x2_t r = __builtin_convertvector(v, bf16x2_t); return __builtin_bit_cast(unsigned, r); }
DI f32x4 mfma(v4u a, v4u b, f32x4 c) { return __builtin_amdgcn_mfma_f32_16x16x32_bf16(__builtin_bit_cast(bf16x8, a), __builtin_bit_cast(bf16x8, b), c, 0, 0, 0); }
DI v4u ld128s(__amdgpu_buffer_rsrc_t rs, int off) { return __builtin_amdgcn_raw_buffer_load_b128(rs, off, 0, AUX_SC1); }
DI v2u ld64s(__amdgpu_buffer_rsrc_t rs, int off) { return __builtin_amdgcn_raw_buffer_load_b64(rs, off, 0, AUX_SC1); }
DI unsigned ld32s(__amdgpu_buffer_rsrc_t rs, int off) { return __builtin_amdgcn_raw_buffer_load_b32(rs, off, 0, AUX_SC1); }
DI void st128s(v4u v, __amdgpu_buffer_rsrc_t rs, int off) { __builtin_amdgcn_raw_buffer_store_b128(v, rs, off, 0, AUX_SC1); }
DI void st64s(v2u v, __amdgpu_buffer_rsrc_t rs, int off) { __builtin_amdgcn_raw_buffer_store_b64(v, rs, off, 0, AUX_SC1); }
DI void st32s(unsigned v, __amdgpu_buffer_rsrc_t rs, int off) { __builtin_amdgcn_raw_buffer_store_b32(v, rs, off, 0, AUX_SC1); }
DI unsigned canon(unsigned x) { return x < 0xFFFFFFFEu ? x : 0xFFFFFFFEu; }
DI unsigned umax(unsigned a, unsigned b) { return a > b ? a : b; }
DI unsigned mx4(v4u v) { return umax(umax(v.x, v.y), umax(v.z, v.w)); }
DI bool ok4(v4u v) { return mx4(v) != SENT; }
DI void lbar() { asm volatile("s_waitcnt lgkmcnt(0)\n\ts_barrier" ::: "memory"); }
DI void vdrain() { asm volatile("s_waitcnt vmcnt(0)" ::: "memory"); }
#define MEMBAR() asm volatile("" ::: "memory")
#define BLD(a, off, rs) asm volatile("buffer_load_dwordx4 %0, %1, %2, 0 offen sc1" : "+v"(a) : "v"(off), "s"(rs) : "memory")
#define BWAIT(n, a) asm volatile("s_waitcnt vmcnt(" #n ")" : "+v"(a) :: "memory")
DI float vmax(float a, float b) { return __builtin_elementwise_maximum(a, b); }
DI float swap32_add(float x) { auto r = __builtin_amdgcn_permlane32_swap(__float_as_uint(x), __float_as_uint(x), false, false); return __uint_as_float(r[0]) + __uint_as_float(r[1]); }
DI float swap16_add(float x) { auto r = __builtin_amdgcn_permlane16_swap(__float_as_uint(x), __float_as_uint(x), false, false); return __uint_as_float(r[0]) + __uint_as_float(r[1]); }
DI float swap32_max(float x) { auto r = __builtin_amdgcn_permlane32_swap(__float_as_uint(x), __float_as_uint(x), false, false); return vmax(__uint_as_float(r[0]), __uint_as_float(r[1])); }
DI float swap16_max(float x) { auto r = __builtin_amdgcn_permlane16_swap(__float_as_uint(x), __float_as_uint(x), false, false); return vmax(__uint_as_float(r[0]), __uint_as_float(r[1])); }
DI float ror8(float x) { return __uint_as_float((unsigned)__builtin_amdgcn_update_dpp(0, (int)__float_as_uint(x), 0x128, 0xf, 0xf, false)); }
DI float ex2(float x) { return __builtin_amdgcn_exp2f(x); }
DI float bflo(unsigned u) { return __uint_as_float(u << 16); }
DI float bfhi(unsigned u) { return __uint_as_float(u & 0xffff0000u); }
DI int xch_q(int slot, int blk, int q) { return (slot * 5 + blk) * 32768 + q * 8192; }
DI int xch_off(int slot, int blk, int cls) { return xch_q(slot, blk, blk == 0 ? cls ^ 2 : cls); }

struct Params {
  const u16* x; const u16* mem0; const u16* Wq; const u16* Wk; const u16* Wv; const u16* Wo;
  const u16* bx; const u16* wqkv; const u16* wo;
  u16* out; unsigned char* ws;
};
struct Ctx { __amdgpu_buffer_rsrc_t rs; GAS u64* cw; u64 tagw; int cls; };

DI void cw_set(const Ctx& c, int idx, unsigned n) { __hip_atomic_store(c.cw + idx, c.tagw | n, RLX_AGENT); }
DI bool cw_ge(const Ctx& c, u64 v, unsigned want) { const u64 d = v - c.tagw; return d <= 255ull && (unsigned)d >= want; }
DI bool cw_tmo(const Ctx& c) { return __builtin_amdgcn_readfirstlane((int)cw_ge(c, __hip_atomic_load(c.cw + CW_TMO, RLX_AGENT), 1u)) != 0; }
DI bool wait_vec(const Ctx& c, int idx, unsigned want, int nap) {
  for (unsigned spins = 0;; ++spins) {
    const u64 v = __hip_atomic_load(c.cw + idx, RLX_AGENT);
    if (__all(cw_ge(c, v, want))) return true;
    if (spins > SPIN_CAP) { cw_set(c, CW_TMO, 1u); return false; }
    if ((spins & 255u) == 255u && cw_tmo(c)) return false;
    if (nap == 1) __builtin_amdgcn_s_sleep(1); else __builtin_amdgcn_s_sleep(4);
  }
}
DI bool wait_prog(const Ctx& c, int idx, unsigned want, unsigned& seen) {
  if (seen >= want) return true;
  const bool ok = wait_vec(c, idx, want, 1);
  unsigned v = (unsigned)(__hip_atomic_load(c.cw + idx, RLX_AGENT) - c.tagw) & 255u;
#pragma unroll
  for (int d = 1; d < 64; d <<= 1) { const unsigned o = (unsigned)__shfl_xor((int)v, d); v = o < v ? o : v; }
  seen = ok ? (unsigned)__builtin_amdgcn_readfirstlane((int)v) : want;
  return ok;
}
DI bool poll128(const Ctx& c, int off, v4u& m) {
  unsigned spins = 0;
  bool good = true;
  do {
    asm volatile("" ::: "memory");
    m = ld128s(c.rs, off);
    if (++spins > SPIN_CAP) { good = false; break; }
  } while (!ok4(m));
  if (!__all(good)) { cw_set(c, CW_TMO, 1u); return false; }
  return true;
}

template <bool ISA>
DI void prep_block(const Params& p, const Ctx& c, unsigned char* lds, int m, int ra, int cb, bool docal, unsigned (&best)[4]) {
  const int tid = threadIdx.x, lane = tid & 63, w = __builtin_amdgcn_readfirstlane(tid >> 6);
  const int h = m & 15, r16 = lane & 15, gq = lane >> 4;
  constexpr int RS = 272, L_R = 0, L_L = 224 * RS;
  const u16* rowsrc = ISA ? p.Wk : p.Wv;
  v4u gr[2][7], gl[2][4];
#pragma unroll
  for (int hf = 0; hf < 2; ++hf) {
#pragma unroll
    for (int i = 0; i < 7; ++i) { const int q = tid + 512 * i; gr[hf][i] = *(const v4u*)(rowsrc + ((size_t)(224 * cb + (q >> 4)) * H + h) * E + 128 * hf + (q & 15) * 8); }
#pragma unroll
    for (int i = 0; i < 4; ++i) {
      const int q = (i < 3 || tid < 256) ? tid + 512 * i : tid;
      if constexpr (ISA) gl[hf][i] = *(const v4u*)(p.Wq + ((size_t)(112 * ra + (q >> 4)) * H + h) * E + 128 * hf + (q & 15) * 8);
      else { const int e = q / 14, ch = q - e * 14; gl[hf][i] = *(const v4u*)(p.Wo + ((size_t)h * E + 128 * hf + e) * D + 112 * ra + ch * 8); }
    }
  }
  f32x4 acc[7][2];
#pragma unroll
  for (int t = 0; t < 7; ++t) { acc[t][0] = f32x4{0, 0, 0, 0}; acc[t][1] = f32x4{0, 0, 0, 0}; }
#pragma unroll
  for (int hf = 0; hf < 2; ++hf) {
    if (hf) lbar();
#pragma unroll
    for (int i = 0; i < 7; ++i) { const int q = tid + 512 * i; *(v4u*)(lds + L_R + (q >> 4) * RS + (q & 15) * 16) = gr[hf][i]; }
#pragma unroll
    for (int i = 0; i < 4; ++i) {
      const int q = (i < 3 || tid < 256) ? tid + 512 * i : tid;
      if constexpr (ISA) *(v4u*)(lds + L_L + (q >> 4) * RS + (q & 15) * 16) = gl[hf][i];
      else { const int e = q / 14, ch = q - e * 14; *(v4u*)(lds + L_L + e * 224 + ch * 16) = gl[hf][i]; }
    }
    lbar();
    if (w < 7) {
      v4u fa[2][2], fb[2][7];
      auto rd = [&](int es, int bf) {
        fa[bf][0] = *(const v4u*)(lds + L_R + (32 * w + r16) * RS + es * 64 + gq * 16); fa[bf][1] = *(const v4u*)(lds + L_R + (32 * w + 16 + r16) * RS + es * 64 + gq * 16);
#pragma unroll
        for (int t = 0; t < 7; ++t) {
          if constexpr (ISA) fb[bf][t] = *(const v4u*)(lds + L_L + (16 * t + r16) * RS + es * 64 + gq * 16);
          else {
            const int q = (lane & 15) >> 2, pp = lane & 3;
            const int base = L_L + (32 * es + 8 * gq + q) * 224 + (16 * t + 4 * pp) * 2;
            typedef short v4s __attribute__((ext_vector_type(4)));
            v4s lo = __builtin_amdgcn_ds_read_tr16_b64_v4i16((LAS v4s*)((LAS unsigned char*)lds + base));
            v4s hi = __builtin_amdgcn_ds_read_tr16_b64_v4i16((LAS v4s*)((LAS unsigned char*)lds + base + 4 * 224));
            v2u l2 = __builtin_bit_cast(v2u, lo), h2 = __builtin_bit_cast(v2u, hi);
            fb[bf][t] = v4u{l2.x, l2.y, h2.x, h2.y};
          }
        }
      };
      rd(0, 0);
#pragma unroll
      for (int es = 0; es < 4; ++es) {
        __builtin_amdgcn_sched_barrier(0);
        if (es < 3) rd(es + 1, (es + 1) & 1);
        __builtin_amdgcn_sched_barrier(0);
#pragma unroll
        for (int t = 0; t < 7; ++t) { acc[t][0] = mfma(fa[es & 1][0], fb[es & 1][t], acc[t][0]); acc[t][1] = mfma(fa[es & 1][1], fb[es & 1][t], acc[t][1]); }
      }
      __builtin_amdgcn_sched_barrier(0);
    } else if (docal) {
#pragma unroll 1
      for (int rep = 0; rep < 8; ++rep) {
        const int q = rep & 3;
        vdrain();
        const unsigned long long t0 = __builtin_amdgcn_s_memtime();
        const unsigned x = ld32s(c.rs, (int)XCH_OFF + (24 + 2 * hf + (rep >> 2)) * 32768 + q * 8192);
        asm volatile("s_waitcnt vmcnt(0)" :: "v"(x) : "memory");
        const unsigned d = (unsigned)(__builtin_amdgcn_s_memtime() - t0);
        if (q == 0) best[0] = d < best[0] ? d : best[0]; else if (q == 1) best[1] = d < best[1] ? d : best[1]; else if (q == 2) best[2] = d < best[2] ? d : best[2]; else best[3] = d < best[3] ? d : best[3];
      }
    }
  }
  if (w < 7) {
    const float sc = ISA ? 0.0625f : 1.0f;
    const int gq2 = (gq >> 1) + 2 * (gq & 1);
#pragma unroll
    for (int t = 0; t < 7; ++t) {
      unsigned x0 = pk2(acc[t][0][0] * sc, acc[t][0][1] * sc), x1 = pk2(acc[t][0][2] * sc, acc[t][0][3] * sc);
      unsigned y0 = pk2(acc[t][1][0] * sc, acc[t][1][1] * sc), y1 = pk2(acc[t][1][2] * sc, acc[t][1][3] * sc);
      auto s0 = __builtin_amdgcn_permlane16_swap(x0, y0, false, false);
      auto s1 = __builtin_amdgcn_permlane16_swap(x1, y1, false, false);
      v4u o = {s0[0], s1[0], s0[1], s1[1]};
      st128s(o, c.rs, (int)(WF_OFF + ((size_t)((m * 28 + 7 * ra + t) * 14 + 7 * cb + w) * 64 + r16 + 16 * gq2) * 16));
    }
  }
}
DI v4u ld_wf(const Ctx& c, int tile, int ks, int lane) { return ld128s(c.rs, (int)(WF_OFF + ((size_t)(tile * 14 + ks) * 64 + lane) * 16)); }
DI v4u ld_mem0(const Params& p, int k, int lane) { return *(const v4u*)(p.mem0 + (lane & 7) * D + 32 * (k + 7 * ((lane >> 3) & 1)) + 8 * (lane >> 4)); }
DI bool wait_all_prep(Ctx& c, int lane) {
  bool ok = true;
#pragma unroll
  for (int k = 0; k < 4; ++k) ok = ok && wait_vec(c, CW_PF + 64 * k + lane, 1u, 1);
  return ok;
}
DI bool wait_class(Ctx& c) {
  const bool ok = wait_vec(c, CW_CLASS, 1u, 1);
  c.cls = __builtin_amdgcn_readfirstlane((int)(__hip_atomic_load(c.cw + CW_CLASS, RLX_AGENT) >> 1) & 3);
  return ok;
}

constexpr int L_MEM = 0;
constexpr int L_C = 8192;
constexpr int L_VX = 16384;
constexpr int L_P = 8192;

DI void role_S(const Params& p, Ctx& c, unsigned char* lds, int h, int g) {
  const int tid = threadIdx.x, lane = tid & 63, w = __builtin_amdgcn_readfirstlane(tid >> 6);
  const int gq = lane >> 4, c16 = lane & 15;
  const int rec = (h * 4 + g) * 512;
  const int recblk = 1 + (rec >> 13), recin = rec & 8191;
  bool alive = true;
  if (w < 4) {
    alive = wait_vec(c, CW_PF + h * 8 + 2 * g + (lane & 1), 1u, 1);
    v4u wa[2][14];
#pragma unroll
    for (int t2 = 0; t2 < 2; ++t2)
#pragma unroll
      for (int ks = 0; ks < 14; ++ks) {
        const int tile = (w == 3 && t2 == 1) ? 6 : 2 * w + t2;
        wa[t2][ks] = ld_wf(c, h * 28 + 7 * g + tile, ks, lane);
      }
    int maddr[2];
#pragma unroll
    for (int t2 = 0; t2 < 2; ++t2) {
      const int tile = (w == 3 && t2 == 1) ? 6 : 2 * w + t2;
      const int a = 112 * g + 16 * tile + 4 * gq, ks = a >> 5, ksq = ks >= 7 ? ks - 7 : ks, hf = ks >= 7 ? 1 : 0;
      maddr[t2] = L_MEM + ksq * 1024 + (((a & 31) >> 3) * 16 + hf * 8 + (c16 & 7)) * 16 + (a & 7) * 2;
    }
    *(v4u*)(lds + L_MEM + w * 1024 + lane * 16) = ld_mem0(p, w, lane);
    alive = wait_all_prep(c, lane) && alive;
    alive = wait_class(c) && alive;
    vdrain();
    for (int s = 0; s < NSEG; ++s) {
      if (s > 0) {
        v4u m;
        alive = poll128(c, xch_off(s & 3, 0, c.cls) + w * 1024 + lane * 16, m) && alive;
        *(v4u*)(lds + L_MEM + w * 1024 + lane * 16) = m;
      }
      lbar();
      v4u mf[7];
#pragma unroll
      for (int k = 0; k < 7; ++k) mf[k] = *(const v4u*)(lds + L_MEM + k * 1024 + lane * 16);
      const v2u mb0 = *(const v2u*)(lds + maddr[0]), mb1 = *(const v2u*)(lds + maddr[1]);
      float v0[4], v1[4];
      f32x4 d2;
      if (w < 3) {
        f32x4 a1 = {0, 0, 0, 0}, a2 = {0, 0, 0, 0}, b1 = {0, 0, 0, 0}, b2 = {0, 0, 0, 0};
#pragma unroll
        for (int k = 0; k < 7; ++k) {
          a1 = mfma(wa[0][k], mf[k], a1); a2 = mfma(wa[0][k + 7], mf[k], a2);
          b1 = mfma(wa[1][k], mf[k], b1); b2 = mfma(wa[1][k + 7], mf[k], b2);
        }
#pragma unroll
        for (int r = 0; r < 4; ++r) { v0[r] = (lane & 8) ? a2[r] : a1[r]; v1[r] = (lane & 8) ? b2[r] : b1[r]; }
        d2 = mfma(v4u{pk2(v0[0], v0[1]), pk2(v0[2], v0[3]), pk2(v1[0], v1[1]), pk2(v1[2], v1[3])}, v4u{mb0.x, mb0.y, mb1.x, mb1.y}, f32x4{0, 0, 0, 0});
      } else {
        f32x4 a1 = {0, 0, 0, 0}, a2 = {0, 0, 0, 0};
#pragma unroll
        for (int k = 0; k < 7; ++k) { a1 = mfma(wa[0][k], mf[k], a1); a2 = mfma(wa[0][k + 7], mf[k], a2); }
#pragma unroll
        for (int r = 0; r < 4; ++r) { v0[r] = (lane & 8) ? a2[r] : a1[r]; v1[r] = 0.f; }
        d2 = mfma(v4u{pk2(v0[0], v0[1]), pk2(v0[2], v0[3]), 0u, 0u}, v4u{mb0.x, mb0.y, 0u, 0u}, f32x4{0, 0, 0, 0});
      }
      {
        f32x4 o;
#pragma unroll
        for (int r = 0; r < 4; ++r) o[r] = swap32_add(d2[r]);
        if (c16 < 8 && gq < 2) *(f32x4*)(lds + L_P + ((w * 2 + gq) * 8 + c16) * 16) = o;
      }
      lbar();
      {
        const int cb = (s * 16 + h) * 56 + 14 * g + 4 * w + (gq >> 1);
        float f[4];
#pragma unroll
        for (int r = 0; r < 4; ++r) f[r] = v0[r] + ror8(v0[r]);
        if (c16 < 8) st64s(v2u{pk2(f[0], f[1]), pk2(f[2], f[3])}, c.rs, (int)(KKM_OFF + (size_t)cb * 128 + c16 * 16 + (gq & 1) * 8));
        if (w < 3) {
#pragma unroll
          for (int r = 0; r < 4; ++r) f[r] = v1[r] + ror8(v1[r]);
          if (c16 < 8) st64s(v2u{pk2(f[0], f[1]), pk2(f[2], f[3])}, c.rs, (int)(KKM_OFF + (size_t)(cb + 2) * 128 + c16 * 16 + (gq & 1) * 8));
        }
      }
      if (!alive) break;
    }
    vdrain();
    lbar();
  } else {
    unsigned kkprog = 0;
    v4u kf[7];
    auto load_kk = [&](int s) -> bool {
      if (!wait_prog(c, CW_PROG + (2 * h + (lane & 1)) * 16, s + 1, kkprog)) return false;
#pragma unroll
      for (int k = 0; k < 7; ++k) kf[k] = ld128s(c.rs, (int)(KKF_OFF + ((size_t)(((s * 16 + h) * 4 + g) * 7 + k) * 64 + lane) * 16));
      return true;
    };
    if (w < 7) *(v4u*)(lds + L_MEM + w * 1024 + lane * 16) = ld_mem0(p, w, lane);
    alive = wait_all_prep(c, lane);
    alive = wait_class(c) && alive;
    if (w == 7) alive = load_kk(0) && alive;
    for (int s = 0; s < NSEG; ++s) {
      const int slot = s & 3;
      if (s > 0 && w < 7) {
        v4u m;
        alive = poll128(c, xch_off(slot, 0, c.cls) + w * 1024 + lane * 16, m) && alive;
        *(v4u*)(lds + L_MEM + w * 1024 + lane * 16) = m;
      }
      lbar();
      if (w == 5 && s > 0 && lane == 0) cw_set(c, CW_PM + (h * 4 + g) * 16, (unsigned)s);
      if (w == 7 && s < NSEG - 1) {
        __builtin_amdgcn_s_setprio(2);
        v4u mf[7];
#pragma unroll
        for (int k = 0; k < 7; ++k) mf[k] = *(const v4u*)(lds + L_MEM + k * 1024 + lane * 16);
        __builtin_amdgcn_sched_barrier(0);
        f32x4 ax = {0, 0, 0, 0}, ay = {0, 0, 0, 0};
#pragma unroll
        for (int k = 0; k < 7; ++k) { if (k & 1) ay = mfma(kf[k], mf[k], ay); else ax = mfma(kf[k], mf[k], ax); }
        float sx[4], a[4]; unsigned t[4];
#pragma unroll
        for (int r = 0; r < 4; ++r) a[r] = ax[r] + ay[r];
        __builtin_amdgcn_sched_barrier(0);
#pragma unroll
        for (int r = 0; r < 4; ++r) t[r] = (unsigned)__builtin_amdgcn_mov_dpp((int)__float_as_uint(a[r]), 0x128, 0xf, 0xf, false);
        __builtin_amdgcn_sched_barrier(0);
#pragma unroll
        for (int r = 0; r < 4; ++r) { auto q = __builtin_amdgcn_permlane32_swap(t[r], __float_as_uint(ay[r]), false, false); t[r] = q[1]; }
        __builtin_amdgcn_sched_barrier(0);
#pragma unroll
        for (int r = 0; r < 4; ++r) sx[r] = a[r] + __uint_as_float(t[r]);
        const float m = swap16_max(vmax(vmax(sx[0], sx[1]), vmax(sx[2], sx[3])));
        const int ro = xch_off(slot, recblk, c.cls) + recin;
        const float mb = -m * LOG2E;
        float e[4];
#pragma unroll
        for (int r = 0; r < 4; ++r) e[r] = ex2(fmaf(sx[r], LOG2E, mb));
        if (c16 < 8 && gq < 2) st64s(v2u{canon(pk2(e[0], e[1])), canon(pk2(e[2], e[3]))}, c.rs, ro + 256 + c16 * 16 + gq * 8);
        const float l = swap16_add((e[0] + e[1]) + (e[2] + e[3]));
        if (c16 < 8 && gq == 0) st64s(v2u{canon(__float_as_uint(m)), canon(__float_as_uint(l))}, c.rs, ro + 384 + c16 * 8);
        __builtin_amdgcn_s_setprio(0);
      }
      lbar();
      if (w == 7 && s + 1 < NSEG - 1) alive = alive && load_kk(s + 1);
      if (w == 4 && s < NSEG - 1) {
        const int i = lane & 7, gg = (lane >> 3) & 1;
        f32x4 t0 = *(const f32x4*)(lds + L_P + ((0 + gg) * 8 + i) * 16), t1 = *(const f32x4*)(lds + L_P + ((2 + gg) * 8 + i) * 16);
        f32x4 t2 = *(const f32x4*)(lds + L_P + ((4 + gg) * 8 + i) * 16), t3 = *(const f32x4*)(lds + L_P + ((6 + gg) * 8 + i) * 16);
        f32x4 o = (t0 + t1) + (t2 + t3);
        const int ro = xch_off(slot, recblk, c.cls) + recin;
        if (lane < 16) st64s(v2u{canon(pk2(o[0], o[1])), canon(pk2(o[2], o[3]))}, c.rs, ro + i * 16 + gg * 8);
        if (lane < 28) st128s(v4u{SENT, SENT, SENT, SENT}, c.rs, xch_off((s + 2) & 3, recblk, c.cls) + recin + lane * 16);
      }
      if (!alive) break;
    }
    lbar();
    if (w == 5 && lane == 0) {
      if (alive) cw_set(c, CW_PM + (h * 4 + g) * 16, (unsigned)NSEG);
      if (h == 0 && g == 0) __hip_atomic_store(c.cw + CW_EPOCH, c.tagw >> 8, RLX_AGENT);
    }
  }
}

DI void role_O(const Params& p, Ctx& c, unsigned char* lds, int oc) {
  const int tid = threadIdx.x, lane = tid & 63, w = __builtin_amdgcn_readfirstlane(tid >> 6);
  const int gq = lane >> 4, c16 = lane & 15;
  bool alive = wait_vec(c, CW_PF + (16 + 2 * w + (lane & 1)) * 8 + 2 * (oc / 7) + ((lane >> 1) & 1), 1u, 1);
  v4u wb[2][14];
#pragma unroll
  for (int hh = 0; hh < 2; ++hh)
#pragma unroll
    for (int ks = 0; ks < 14; ++ks) wb[hh][ks] = ld_wf(c, 448 + (2 * w + hh) * 28 + oc, ks, lane);
  if (w < 7) *(v4u*)(lds + L_MEM + w * 1024 + lane * 16) = ld_mem0(p, w, lane);
  const int half = oc >= 14 ? 1 : 0;
  unsigned vprog = 0;
  auto load_vv = [&](int s) -> bool {
    const bool ok = wait_prog(c, CW_PROG + (32 + 2 * (lane & 15) + half) * 16, s + 1, vprog);
#pragma unroll
    for (int q = 0; q < 4; ++q) {
      const int src = (int)(VVF_OFF + ((size_t)((s * 28 + oc) * 16 + 4 * q) * 64 + lane) * 16);
      const int dst = __builtin_amdgcn_readfirstlane(L_VX + (s & 1) * 16384 + q * 4096);
      asm volatile("s_mov_b32 m0, %0\n\ts_nop 0\n\tbuffer_load_dwordx4 %1, %2, 0 offen sc1 lds\n\tbuffer_load_dwordx4 %1, %2, 0 offen offset:1024 sc1 lds\n\t"
                   "buffer_load_dwordx4 %1, %2, 0 offen offset:2048 sc1 lds\n\tbuffer_load_dwordx4 %1, %2, 0 offen offset:3072 sc1 lds"
                   :: "s"(dst), "v"(src), "s"(c.rs) : "memory", "m0");
    }
    return ok;
  };
  vdrain();
  alive = wait_all_prep(c, lane) && alive;
  alive = wait_class(c) && alive;
  if (w == 7) { alive = load_vv(0) && alive; vdrain(); }
  const int rh = 2 * w + ((lane >> 3) & 1), ri = lane & 7;
  const int rec = (rh * 4 + gq) * 512, recblk = 1 + (rec >> 13), recin = rec & 8191;
  const int oks = oc >> 1, oksq = oks >= 7 ? oks - 7 : oks, ohf = oks >= 7 ? 1 : 0;
  v4u sel;
#pragma unroll
  for (int k = 0; k < 4; ++k) sel[k] = (c16 & 7) == 2 * k ? 0x00003F80u : ((c16 & 7) == 2 * k + 1 ? 0x3F800000u : 0u);
  const bool diag = (gq >> 1) == (c16 >> 3);
  v2u zz = {0u, 0u};
  for (int s = 0; s < NSEG; ++s) {
    const int slot = s & 3;
    if (w < 7 && s > 0) {
      v4u m;
      __builtin_amdgcn_s_sleep(NAP_OM);
      alive = alive && poll128(c, xch_off(slot, 0, c.cls) + w * 1024 + lane * 16, m);
      *(v4u*)(lds + L_MEM + w * 1024 + lane * 16) = m;
    }
    lbar();
    if (s > 0) st64s(zz, c.rs, (int)(VMF_OFF + ((size_t)(((s - 1) * 28 + oc) * 8 + w) * 64 + lane) * 8));
    if (w == 7 && s + 1 < NSEG - 1) alive = load_vv(s + 1) && alive;
    v4u mf[7];
#pragma unroll
    for (int k = 0; k < 7; ++k) mf[k] = *(const v4u*)(lds + L_MEM + k * 1024 + lane * 16);
    v4u vx[2];
#pragma unroll
    for (int hh = 0; hh < 2; ++hh) vx[hh] = *(const v4u*)(lds + L_VX + (s & 1) * 16384 + (2 * w + hh) * 1024 + lane * 16);
    {
      float z[2][4];
#pragma unroll
      for (int hh = 0; hh < 2; ++hh) {
        f32x4 aA = {0, 0, 0, 0}, aB = {0, 0, 0, 0};
#pragma unroll
        for (int k = 0; k < 7; ++k) { aA = mfma(mf[k], wb[hh][k], aA); aB = mfma(mf[k], wb[hh][k + 7], aB); }
#pragma unroll
        for (int r = 0; r < 4; ++r) z[hh][r] = swap32_add(gq < 2 ? aA[r] : aB[r]);
      }
      zz = gq < 2 ? v2u{pk2(z[0][0], z[0][1]), pk2(z[0][2], z[0][3])} : v2u{pk2(z[1][0], z[1][1]), pk2(z[1][2], z[1][3])};
    }
    asm volatile("" :: "v"(zz));
    if (s < NSEG - 1) {
      const int ro = xch_off(slot, recblk, c.cls) + recin;
      v4u c1 = {SENT, SENT, SENT, SENT}, c2; v2u c3;
      for (unsigned spins = 0;; ++spins) {
        asm volatile("" ::: "memory");
        c3 = ld64s(c.rs, ro + 384 + ri * 8); c2 = ld128s(c.rs, ro + 256 + ri * 16);
        if (__all(umax(mx4(c2), umax(c3.x, c3.y)) != SENT)) break;
        if (spins > SPIN_CAP) { cw_set(c, CW_TMO, 1u); alive = false; break; }
      }
      const int so = ro + ri * 16;
      BLD(c1, so, c.rs);
      __builtin_amdgcn_sched_barrier(0);
      const float mg = __uint_as_float(c3.x), lg = __uint_as_float(c3.y);
      const float mx = swap32_max(swap16_max(mg));
      const float f = ex2((mg - mx) * LOG2E);
      float ef[8];
#pragma unroll
      for (int k = 0; k < 4; ++k) { ef[2 * k] = bflo(c2[k]); ef[2 * k + 1] = bfhi(c2[k]); }
      const float lx = swap32_add(swap16_add(f * lg));
      v4u ax;
#pragma unroll
      for (int k = 0; k < 4; ++k) ax[k] = pk2(ef[2 * k] * f, ef[2 * k + 1] * f);
      const f32x4 oa = mfma(vx[0], ax, f32x4{0, 0, 0, 0}), ob = mfma(vx[1], ax, f32x4{0, 0, 0, 0});
      f32x4 ox;
#pragma unroll
      for (int r = 0; r < 4; ++r) ox[r] = c16 < 8 ? oa[r] : ob[r];
      const float mxl = mx * LOG2E;
      asm volatile("" :: "v"(ox), "v"(lx), "v"(mxl));
#pragma nounroll
      for (unsigned spins = 0;; ++spins) {
        BLD(c1, so, c.rs);
        BWAIT(1, c1);
        if (__all(ok4(c1))) break;
        if (spins > SPIN_CAP) { cw_set(c, CW_TMO, 1u); alive = false; break; }
      }
      const f32x4 sc = mfma(sel, c1, f32x4{0, 0, 0, 0});
      const float ml = swap16_max(vmax(vmax(sc[0], sc[1]), vmax(sc[2], sc[3]))) * LOG2E;
      const float mb = -vmax(ml, mxl);
      float e[4];
#pragma unroll
      for (int r = 0; r < 4; ++r) e[r] = ex2(fmaf(sc[r], LOG2E, mb));
      const float cx = ex2(mxl + mb);
      const float l = fmaf(cx, lx, swap16_add((e[0] + e[1]) + (e[2] + e[3])));
      const float rl = __builtin_amdgcn_rcpf(l);
      const float rm = diag ? rl : 0.f, kx = cx * rl;
      f32x4 o = mfma(v4u{zz.x, zz.y, 0u, 0u}, v4u{pk2(e[0] * rm, e[1] * rm), pk2(e[2] * rm, e[3] * rm), 0u, 0u}, ox * kx);
#pragma unroll
      for (int r = 0; r < 4; ++r) o[r] += ror8(o[r]);
      if (c16 < 8) *(f32x4*)(lds + L_C + ((w * 8 + c16) * 16 + 4 * gq) * 4) = o;
      lbar();
      BWAIT(0, c1);
      if (w == 7) {
        const int i = (lane >> 2) & 7, mp = (lane >> 5) * 4 + (lane & 3);
        f32x2 t[8];
#pragma unroll
        for (int k = 0; k < 8; ++k) t[k] = *(const f32x2*)(lds + L_C + ((k * 8 + i) * 16 + 2 * mp) * 4);
        f32x2 sum = ((t[0] + t[1]) + (t[2] + t[3])) + ((t[4] + t[5]) + (t[6] + t[7]));
        const int pos = oksq * 1024 + ((2 * (oc & 1) + (lane >> 5)) * 16 + ohf * 8 + i) * 16 + (lane & 3) * 4;
        st32s(canon(pk2(sum[0], sum[1])), c.rs, xch_off((s + 1) & 3, 0, c.cls) + pos);
        st32s(SENT, c.rs, xch_off((s + 3) & 3, 0, c.cls) + pos);
        if (s > 0 && lane == 0) cw_set(c, CW_PM + (64 + oc) * 16, (unsigned)s);
      }
    }
    if (!alive) break;
  }
  st64s(zz, c.rs, (int)(VMF_OFF + ((size_t)(((NSEG - 1) * 28 + oc) * 8 + w) * 64 + lane) * 8));
  vdrain();
  lbar();
  if (w == 7 && alive && lane == 0) cw_set(c, CW_PM + (64 + oc) * 16, (unsigned)NSEG);
}

constexpr int XRS = 912, XB = 32 * XRS;
DI int xrow_addr(int q) { return (q / 56) * XRS + (q % 56) * 16; }
DI void role_KV(const Params& p, Ctx& c, unsigned char* lds, int hm2) {
  const int tid = threadIdx.x, lane = tid & 63, w = __builtin_amdgcn_readfirstlane(tid >> 6);
  const int gq = lane >> 4, c16 = lane & 15;
  const int m = hm2 >> 1, half = hm2 & 1;
  const bool iskk = m < 16;
  const int h = m & 15;
  constexpr int SPB = 28 * 1024, L_SP = 2 * XB;
  constexpr int EARLY = 2;
  if (w == 7) {
    auto reduce = [&](int s) {
      const unsigned char* sp = lds + L_SP + (s & 1) * SPB + lane * 16;
#pragma unroll
      for (int qt = 0; qt < 2; ++qt) {
        f32x4 a0 = *(const f32x4*)(sp + qt * 1024), a1 = *(const f32x4*)(sp + (2 + qt) * 1024);
#pragma unroll
        for (int w2 = 1; w2 < 7; ++w2) { a0 += *(const f32x4*)(sp + (w2 * 4 + qt) * 1024); a1 += *(const f32x4*)(sp + (w2 * 4 + 2 + qt) * 1024); }
        st128s(v4u{pk2(a0[0], a0[1]), pk2(a0[2], a0[3]), pk2(a1[0], a1[1]), pk2(a1[2], a1[3])}, c.rs, (int)(SXX_OFF + ((size_t)(((s * 16 + h) * 2 + half) * 2 + qt) * 64 + lane) * 16));
      }
    };
    const u16* xs = p.x + lane * 8;
    v4u t[28];
#pragma unroll
    for (int f = 0; f < 28; ++f) t[f] = *(const v4u*)(xs + f * 512);
#pragma unroll
    for (int f = 0; f < 28; ++f) *(v4u*)(lds + xrow_addr(f * 64 + lane)) = t[f];
    for (int s = 0; s < NSEG; ++s) {
      lbar();
      const int sn = s + 1 < NSEG ? s + 1 : s;
#pragma unroll
      for (int f = 0; f < 28; ++f) t[f] = *(const v4u*)(xs + (size_t)sn * (32 * D) + f * 512);
      if (iskk && s > 0) reduce(s - 1);
#pragma unroll
      for (int f = 0; f < 28; ++f) *(v4u*)(lds + ((s + 1) & 1) * XB + xrow_addr(f * 64 + lane)) = t[f];
      vdrain();
      lbar();
      if (lane == 0 && s != EARLY) cw_set(c, CW_PROG + hm2 * 16, (unsigned)(s < EARLY ? s + 1 : s));
    }
    lbar();
    if (lane == 0) cw_set(c, CW_PROG + hm2 * 16, (unsigned)NSEG);
    if (iskk) {
      reduce(NSEG - 1);
      vdrain();
      if (lane == 0) cw_set(c, CW_PROG + hm2 * 16, (unsigned)(NSEG + 2));
    }
    return;
  }
  wait_vec(c, CW_PF + m * 8 + 4 * half + (lane & 3), 1u, 1);
  v4u wt[2][14];
#pragma unroll
  for (int t2 = 0; t2 < 2; ++t2)
#pragma unroll
    for (int ks = 0; ks < 14; ++ks) wt[t2][ks] = ld_wf(c, m * 28 + 14 * half + 2 * w + t2, ks, lane);
  vdrain();
  for (int s = 0; s < NSEG; ++s) {
    lbar();
    const unsigned char* xb = lds + (s & 1) * XB;
    const unsigned char* xf0 = xb + c16 * XRS + gq * 16;
    f32x4 acc[2][2];
#pragma unroll
    for (int a = 0; a < 2; ++a) { acc[a][0] = f32x4{0, 0, 0, 0}; acc[a][1] = f32x4{0, 0, 0, 0}; }
    if (iskk) {
#pragma unroll
      for (int ks = 0; ks < 14; ++ks)
#pragma unroll
        for (int rt = 0; rt < 2; ++rt) {
          v4u xf = *(const v4u*)(xf0 + rt * 16 * XRS + ks * 64);
          acc[rt][0] = mfma(wt[0][ks], xf, acc[rt][0]); acc[rt][1] = mfma(wt[1][ks], xf, acc[rt][1]);
        }
      const int gq2 = (gq >> 1) + 2 * (gq & 1);
      v4u kf[2];
#pragma unroll
      for (int rt = 0; rt < 2; ++rt) {
        unsigned x0 = pk2(acc[rt][0][0], acc[rt][0][1]), x1 = pk2(acc[rt][0][2], acc[rt][0][3]);
        unsigned y0 = pk2(acc[rt][1][0], acc[rt][1][1]), y1 = pk2(acc[rt][1][2], acc[rt][1][3]);
        auto s0 = __builtin_amdgcn_permlane16_swap(x0, y0, false, false);
        auto s1 = __builtin_amdgcn_permlane16_swap(x1, y1, false, false);
        kf[rt] = v4u{s0[0], s1[0], s0[1], s1[1]};
      }
      vdrain();
#pragma unroll
      for (int rt = 0; rt < 2; ++rt) {
        const int g = c16 >> 2, rowq = (c16 & 3) + 4 * rt;
        st128s(kf[rt], c.rs, (int)(KKF_OFF + ((size_t)(((s * 16 + h) * 4 + g) * 7 + w) * 64 + rowq + 8 * half + 16 * gq2) * 16));
      }
#pragma unroll
      for (int qt = 0; qt < 2; ++qt) {
        const v4u xq = *(const v4u*)(xb + (16 * qt + c16) * XRS + (7 * half + w) * 64 + gq2 * 16);
#pragma unroll
        for (int kt = 0; kt < 2; ++kt)
          *(f32x4*)(lds + L_SP + (s & 1) * SPB + (w * 4 + kt * 2 + qt) * 1024 + lane * 16) = mfma(kf[kt], xq, f32x4{0, 0, 0, 0});
      }
    } else {
#pragma unroll
      for (int ks = 0; ks < 14; ++ks)
#pragma unroll
        for (int rt = 0; rt < 2; ++rt) {
          v4u xf = *(const v4u*)(xf0 + rt * 16 * XRS + ks * 64);
          acc[rt][0] = mfma(xf, wt[0][ks], acc[rt][0]); acc[rt][1] = mfma(xf, wt[1][ks], acc[rt][1]);
        }
      v4u o[2];
#pragma unroll
      for (int t2 = 0; t2 < 2; ++t2) o[t2] = v4u{pk2(acc[0][t2][0], acc[0][t2][1]), pk2(acc[0][t2][2], acc[0][t2][3]), pk2(acc[1][t2][0], acc[1][t2][1]), pk2(acc[1][t2][2], acc[1][t2][3])};
      vdrain();
#pragma unroll
      for (int t2 = 0; t2 < 2; ++t2) st128s(o[t2], c.rs, (int)(VVF_OFF + ((size_t)((s * 28 + 14 * half + 2 * w + t2) * 16 + h) * 64 + lane) * 16));
    }
    if (s < EARLY) vdrain();
    lbar();
  }
  vdrain();
  lbar();
}

constexpr int LT_X = 0, LT_PX = 30 * 1024, LT_PM = 62 * 1024;
DI void task_T(const Params& p, const Ctx& c, unsigned char* lds, int seg, int cg) {
  const int tid = threadIdx.x, lane = tid & 63, w = __builtin_amdgcn_readfirstlane(tid >> 6);
  const int gq = lane >> 4, c16 = lane & 15;
  const int oc = 7 * cg + (w < 7 ? w : 0);
#pragma unroll
  for (int i = 0; i < 4; ++i) {
    const int q = tid + 512 * i;
    if (q < 1792) *(v4u*)(lds + LT_X + xrow_addr(q)) = *(const v4u*)(p.x + (size_t)seg * (32 * D) + q * 8);
  }
  if (w == 7) wait_vec(c, CW_PROG + lane * 16, (unsigned)seg + (lane < 32 ? 2u : 1u), 4);
  lbar();
  v4u sx[2][2][2], vv[16];
#pragma unroll
  for (int hh = 0; hh < 2; ++hh)
#pragma unroll
    for (int hf = 0; hf < 2; ++hf)
#pragma unroll
      for (int qt = 0; qt < 2; ++qt) sx[hh][hf][qt] = ld128s(c.rs, (int)(SXX_OFF + ((size_t)(((seg * 16 + 2 * w + hh) * 2 + hf) * 2 + qt) * 64 + lane) * 16));
  if (w < 7) {
#pragma unroll
    for (int h = 0; h < 16; ++h) vv[h] = ld128s(c.rs, (int)(VVF_OFF + ((size_t)((seg * 28 + oc) * 16 + h) * 64 + lane) * 16));
  } else {
    wait_vec(c, CW_PM + (cg * 16 + (seg & 15)) * 16, seg + 1, 4);
    wait_vec(c, CW_PM + lane * 16, seg + 1, 1);
    wait_vec(c, CW_PM + (64 + (lane < 28 ? lane : 0)) * 16, seg + 1, 1);
  }
  lbar();
  v4u kf[14]; v2u vm[8];
#pragma unroll
  for (int ks = 0; ks < 14; ++ks) kf[ks] = ld128s(c.rs, (int)(KKM_OFF + (size_t)((seg * 16 + 2 * w + (c16 >> 3)) * 56 + 4 * ks + gq) * 128 + (c16 & 7) * 16));
  if (w < 7) {
#pragma unroll
    for (int pr = 0; pr < 8; ++pr) vm[pr] = ld64s(c.rs, (int)(VMF_OFF + ((size_t)((seg * 28 + oc) * 8 + pr) * 64 + lane) * 8));
  }
#pragma unroll
  for (int qt = 0; qt < 2; ++qt) {
    const unsigned char* xq = lds + LT_X + (16 * qt + c16) * XRS + gq * 16;
    f32x4 am = {0, 0, 0, 0}, am2 = {0, 0, 0, 0};
#pragma unroll
    for (int ks = 0; ks < 14; ks += 2) {
      am = mfma(kf[ks], *(const v4u*)(xq + ks * 64), am);
      am2 = mfma(kf[ks + 1], *(const v4u*)(xq + ks * 64 + 64), am2);
    }
    am += am2;
    const int q = 16 * qt + c16;
    float sc[2][8], mxh[2];
#pragma unroll
    for (int hh = 0; hh < 2; ++hh) {
      float m = (gq >> 1) == hh ? vmax(vmax(am[0], am[1]), vmax(am[2], am[3])) : -1e9f;
#pragma unroll
      for (int k = 0; k < 4; ++k) {
        const unsigned u0 = sx[hh][0][qt][k], u1 = sx[hh][1][qt][k];
        const int key = (k < 2 ? 4 * gq + 2 * k : 16 + 4 * gq + 2 * (k - 2));
        sc[hh][2 * k] = key <= q ? bflo(u0) + bflo(u1) : -1e9f;
        sc[hh][2 * k + 1] = key + 1 <= q ? bfhi(u0) + bfhi(u1) : -1e9f;
        m = vmax(m, vmax(sc[hh][2 * k], sc[hh][2 * k + 1]));
      }
      mxh[hh] = swap32_max(swap16_max(m)) * LOG2E;
    }
    float em[4], lh[2];
    const float mm = gq < 2 ? mxh[0] : mxh[1];
#pragma unroll
    for (int r = 0; r < 4; ++r) em[r] = ex2(fmaf(am[r], LOG2E, -mm));
    const float lm = (em[0] + em[1]) + (em[2] + em[3]);
#pragma unroll
    for (int hh = 0; hh < 2; ++hh) {
      float l = (gq >> 1) == hh ? lm : 0.f;
#pragma unroll
      for (int k = 0; k < 8; ++k) { sc[hh][k] = ex2(fmaf(sc[hh][k], LOG2E, -mxh[hh])); l += sc[hh][k]; }
      lh[hh] = __builtin_amdgcn_rcpf(swap32_add(swap16_add(l)));
      *(v4u*)(lds + LT_PX + ((2 * w + hh) * 2 + qt) * 1024 + lane * 16) =
          v4u{pk2(sc[hh][0] * lh[hh], sc[hh][1] * lh[hh]), pk2(sc[hh][2] * lh[hh], sc[hh][3] * lh[hh]), pk2(sc[hh][4] * lh[hh], sc[hh][5] * lh[hh]), pk2(sc[hh][6] * lh[hh], sc[hh][7] * lh[hh])};
    }
    const float rm = gq < 2 ? lh[0] : lh[1];
    *(v2u*)(lds + LT_PM + (w * 2 + qt) * 512 + lane * 8) = v2u{pk2(em[0] * rm, em[1] * rm), pk2(em[2] * rm, em[3] * rm)};
  }
  lbar();
  if (w < 7) {
    f32x4 o[2] = {f32x4{0, 0, 0, 0}, f32x4{0, 0, 0, 0}};
#pragma unroll
    for (int h = 0; h < 16; ++h)
#pragma unroll
      for (int qt = 0; qt < 2; ++qt) o[qt] = mfma(vv[h], *(const v4u*)(lds + LT_PX + (h * 2 + qt) * 1024 + lane * 16), o[qt]);
#pragma unroll
    for (int i = 0; i < 4; ++i)
#pragma unroll
      for (int qt = 0; qt < 2; ++qt) {
        const v2u b0 = *(const v2u*)(lds + LT_PM + ((2 * i) * 2 + qt) * 512 + lane * 8), b1 = *(const v2u*)(lds + LT_PM + ((2 * i + 1) * 2 + qt) * 512 + lane * 8);
        o[qt] = mfma(v4u{vm[2 * i].x, vm[2 * i].y, vm[2 * i + 1].x, vm[2 * i + 1].y}, v4u{b0.x, b0.y, b1.x, b1.y}, o[qt]);
      }
#pragma unroll
    for (int qt = 0; qt < 2; ++qt)
      *(v2u*)(p.out + (size_t)(32 * seg + 16 * qt + c16) * (2 * D) + 16 * oc + 4 * gq) = v2u{pk2(o[qt][0], o[qt][1]), pk2(o[qt][2], o[qt][3])};
  }
  lbar();
}

DI unsigned phi2(float a, float b) {
  const unsigned t = pk2(a, b);
  const float x = bflo(t), y = bfhi(t);
  const unsigned u = pk2(x > 0.f ? x + 1.f : __expf(x), y > 0.f ? y + 1.f : __expf(y));
  return pk2(bflo(u) + 1e-6f, bfhi(u) + 1e-6f);
}
constexpr int LB_W = 0, LB_R = 448 * 192;
DI v4u wfrag(const unsigned char* lds, int off) {
  typedef short v4s __attribute__((ext_vector_type(4)));
  v4s lo = __builtin_amdgcn_ds_read_tr16_b64_v4i16((LAS v4s*)((LAS unsigned char*)lds + off));
  v4s hi = __builtin_amdgcn_ds_read_tr16_b64_v4i16((LAS v4s*)((LAS unsigned char*)lds + off + 4 * 192));
  v2u l2 = __builtin_bit_cast(v2u, lo), h2 = __builtin_bit_cast(v2u, hi);
  return v4u{l2.x, l2.y, h2.x, h2.y};
}
DI void task_B1(const Params& p, const Ctx& c, unsigned char* lds, int h, int tq) {
  const int tid = threadIdx.x, lane = tid & 63, w = __builtin_amdgcn_readfirstlane(tid >> 6);
  const int gq = lane >> 4, c16 = lane & 15;
#pragma unroll 7
  for (int i = 0; i < 21; ++i) {
    const int idx = tid + 512 * i, k = idx / 24, rem = idx - k * 24, m = rem >> 3, sl = rem & 7;
    v2u v = {0u, 0u};
    if (sl < 7) v = *(const v2u*)(p.wqkv + (size_t)k * (3 * D) + m * D + h * 28 + sl * 4);
    *(v2u*)(lds + LB_W + k * 192 + m * 64 + sl * 8) = v;
  }
  if (w < 7) {
    const int nt = 7 * tq + w;
    unsigned e8[8];
#pragma unroll
    for (int i = 0; i < 8; ++i) { const int e = (i < 4 ? 4 * gq + i : 12 + 4 * gq + i); e8[i] = e < 28 ? (unsigned)p.wo[(size_t)(28 * h + e) * D + 16 * nt + c16] : 0u; }
    st128s(v4u{e8[0] | e8[1] << 16, e8[2] | e8[3] << 16, e8[4] | e8[5] << 16, e8[6] | e8[7] << 16}, c.rs, (int)(WOF_OFF + ((size_t)(h * 28 + nt) * 64 + lane) * 16));
  }
  lbar();
  const int t0 = 512 * tq + 64 * w;
  const u16* xr = p.bx + (size_t)(t0 + c16) * D + 8 * gq;
  const int wb = LB_W + (8 * gq + (c16 >> 2)) * 192 + (lane & 3) * 8;
  f32x4 aq[4][2], ak[4][2], av[4][2];
#pragma unroll
  for (int rt = 0; rt < 4; ++rt)
#pragma unroll
    for (int dt = 0; dt < 2; ++dt) { aq[rt][dt] = f32x4{0, 0, 0, 0}; ak[rt][dt] = f32x4{0, 0, 0, 0}; av[rt][dt] = f32x4{0, 0, 0, 0}; }
  v4u xn[4];
#pragma unroll
  for (int rt = 0; rt < 4; ++rt) xn[rt] = *(const v4u*)(xr + (size_t)(16 * rt) * D);
#pragma unroll 1
  for (int ks = 0; ks < 14; ++ks) {
    v4u x[4];
#pragma unroll
    for (int rt = 0; rt < 4; ++rt) x[rt] = xn[rt];
    const int kn = ks + 1 < 14 ? ks + 1 : ks;
#pragma unroll
    for (int rt = 0; rt < 4; ++rt) xn[rt] = *(const v4u*)(xr + (size_t)(16 * rt) * D + 32 * kn);
#pragma unroll
    for (int dt = 0; dt < 2; ++dt) {
      const v4u wq = wfrag(lds, wb + ks * 32 * 192 + dt * 32), wk = wfrag(lds, wb + ks * 32 * 192 + 64 + dt * 32), wv = wfrag(lds, wb + ks * 32 * 192 + 128 + dt * 32);
#pragma unroll
      for (int rt = 0; rt < 4; ++rt) { aq[rt][dt] = mfma(wq, x[rt], aq[rt][dt]); ak[rt][dt] = mfma(x[rt], wk, ak[rt][dt]); av[rt][dt] = mfma(x[rt], wv, av[rt][dt]); }
    }
  }
#pragma unroll
  for (int rt = 0; rt < 4; ++rt)
    st128s(v4u{phi2(aq[rt][0][0], aq[rt][0][1]), phi2(aq[rt][0][2], aq[rt][0][3]), phi2(aq[rt][1][0], aq[rt][1][1]), phi2(aq[rt][1][2], aq[rt][1][3])}, c.rs,
           (int)(PQ_OFF + ((size_t)((t0 / 16 + rt) * 16 + h) * 64 + lane) * 16));
  f32x4 kv[2][2] = {{f32x4{0, 0, 0, 0}, f32x4{0, 0, 0, 0}}, {f32x4{0, 0, 0, 0}, f32x4{0, 0, 0, 0}}};
#pragma unroll
  for (int rp = 0; rp < 2; ++rp) {
    v4u pk[2], vf[2];
#pragma unroll
    for (int dt = 0; dt < 2; ++dt) {
      const f32x4 k0 = ak[2 * rp][dt], k1 = ak[2 * rp + 1][dt], v0 = av[2 * rp][dt], v1 = av[2 * rp + 1][dt];
      pk[dt] = v4u{phi2(k0[0], k0[1]), phi2(k0[2], k0[3]), phi2(k1[0], k1[1]), phi2(k1[2], k1[3])};
      vf[dt] = v4u{pk2(v0[0], v0[1]), pk2(v0[2], v0[3]), pk2(v1[0], v1[1]), pk2(v1[2], v1[3])};
    }
    if (c16 >= 12) pk[1] = v4u{0u, 0u, 0u, 0u};
    if (c16 == 12) vf[1] = v4u{0x3F803F80u, 0x3F803F80u, 0x3F803F80u, 0x3F803F80u};
#pragma unroll
    for (int dt = 0; dt < 2; ++dt)
#pragma unroll
      for (int et = 0; et < 2; ++et) kv[dt][et] = mfma(pk[dt], vf[et], kv[dt][et]);
  }
#pragma unroll
  for (int t = 0; t < 4; ++t) *(f32x4*)(lds + LB_R + (w * 4 + t) * 1024 + lane * 16) = kv[t >> 1][t & 1];
  lbar();
  if (w < 4) {
    f32x4 a = *(const f32x4*)(lds + LB_R + w * 1024 + lane * 16);
#pragma unroll
    for (int w2 = 1; w2 < 8; ++w2) a += *(const f32x4*)(lds + LB_R + (w2 * 4 + w) * 1024 + lane * 16);
    st128s(__builtin_bit_cast(v4u, a), c.rs, (int)(KVP_OFF + ((size_t)((h * 4 + tq) * 4 + w) * 64 + lane) * 16));
  }
  vdrain();
  lbar();
  if (tid == 0) cw_set(c, CW_B1 + h * 4 + tq, 1u);
}

constexpr int LB_OF = 0;
DI void task_B2(const Params& p, const Ctx& c, unsigned char* lds, int tt) {
  const int tid = threadIdx.x, lane = tid & 63, w = __builtin_amdgcn_readfirstlane(tid >> 6);
  const int gq = lane >> 4;
  const int c16 = lane & 15;
  if (w == 0) wait_vec(c, CW_B1 + lane, 1u, 4);
  lbar();
  v4u pq[2][4];
#pragma unroll
  for (int hh = 0; hh < 2; ++hh)
#pragma unroll
    for (int t = 0; t < 4; ++t) pq[hh][t] = ld128s(c.rs, (int)(PQ_OFF + ((size_t)((4 * tt + t) * 16 + 2 * w + hh) * 64 + lane) * 16));
  v4u kva[2][2];
#pragma unroll
  for (int hh = 0; hh < 2; ++hh)
#pragma unroll
    for (int et = 0; et < 2; ++et) {
      f32x4 d0 = {0, 0, 0, 0}, d1 = {0, 0, 0, 0};
#pragma unroll
      for (int tq = 0; tq < 4; ++tq) {
        d0 += __builtin_bit_cast(f32x4, ld128s(c.rs, (int)(KVP_OFF + ((size_t)(((2 * w + hh) * 4 + tq) * 4 + et) * 64 + lane) * 16)));
        d1 += __builtin_bit_cast(f32x4, ld128s(c.rs, (int)(KVP_OFF + ((size_t)(((2 * w + hh) * 4 + tq) * 4 + 2 + et) * 64 + lane) * 16)));
      }
      kva[hh][et] = v4u{pk2(d0[0], d0[1]), pk2(d0[2], d0[3]), pk2(d1[0], d1[1]), pk2(d1[2], d1[3])};
    }
#pragma unroll
  for (int hh = 0; hh < 2; ++hh)
#pragma unroll
    for (int t = 0; t < 4; ++t) {
      const f32x4 n0 = mfma(kva[hh][0], pq[hh][t], f32x4{0, 0, 0, 0}), n1 = mfma(kva[hh][1], pq[hh][t], f32x4{0, 0, 0, 0});
      float den = swap32_add(swap16_add(gq == 3 ? n1[0] : 0.f));
      den = bflo(pk2(bflo(pk2(den, 0.f)) + 1e-6f, 0.f));
      const float rd = 1.f / den;
      const unsigned a0 = pk2(n0[0], n0[1]), a1 = pk2(n0[2], n0[3]), b0 = pk2(n1[0], n1[1]), b1 = pk2(n1[2], n1[3]);
      v4u o = {pk2(bflo(a0) * rd, bfhi(a0) * rd), pk2(bflo(a1) * rd, bfhi(a1) * rd), pk2(bflo(b0) * rd, bfhi(b0) * rd), pk2(bflo(b1) * rd, bfhi(b1) * rd)};
      if (gq == 3) { o.z = 0u; o.w = 0u; }
      *(v4u*)(lds + LB_OF + ((2 * w + hh) * 4 + t) * 1024 + lane * 16) = o;
    }
  lbar();
  f32x4 acc[4][4];
#pragma unroll
  for (int j = 0; j < 4; ++j)
#pragma unroll
    for (int t = 0; t < 4; ++t) acc[j][t] = f32x4{0, 0, 0, 0};
  const int nj = w < 4 ? 4 : 3;
#pragma unroll 2
  for (int h = 0; h < 16; ++h) {
    v4u a[4], b[4];
#pragma unroll
    for (int j = 0; j < 4; ++j) a[j] = ld128s(c.rs, (int)(WOF_OFF + ((size_t)(h * 28 + (j < nj ? w + 8 * j : w)) * 64 + lane) * 16));
#pragma unroll
    for (int t = 0; t < 4; ++t) b[t] = *(const v4u*)(lds + LB_OF + (h * 4 + t) * 1024 + lane * 16);
#pragma unroll
    for (int j = 0; j < 4; ++j)
#pragma unroll
      for (int t = 0; t < 4; ++t) acc[j][t] = mfma(a[j], b[t], acc[j][t]);
  }
#pragma unroll
  for (int j = 0; j < 4; ++j)
    if (j < nj) {
#pragma unroll
      for (int t = 0; t < 4; ++t)
        *(v2u*)(p.out + (size_t)(64 * tt + 16 * t + c16) * (2 * D) + D + 16 * (w + 8 * j) + 4 * gq) = v2u{pk2(acc[j][t][0], acc[j][t][1]), pk2(acc[j][t][2], acc[j][t][3])};
    }
  lbar();
}

DI void role_W(const Params& p, const Ctx& c, unsigned char* lds, int wid) {
  task_B1(p, c, lds, wid >> 2, wid & 3);
  if ((wid & 1) == 0) task_B2(p, c, lds, wid >> 1);
#pragma unroll 1
  for (int k = 0; k < 4; ++k) { task_T(p, c, lds, 16 * k + (wid >> 2), wid & 3); }
}

__global__ void __launch_bounds__(512, 2) k_main(Params p) {
  extern __shared__ __attribute__((aligned(16))) unsigned char lds[];
  const int b = blockIdx.x, label = b & 7, rank = b >> 3;
  const int tid = threadIdx.x, lane = tid & 63, w = __builtin_amdgcn_readfirstlane(tid >> 6);
  Ctx c;
  c.rs = __builtin_amdgcn_make_buffer_rsrc(p.ws, 0, (int)WS_USED, 0x00020000);
  c.cw = (GAS u64*)(p.ws + CTRL_OFF);
  c.cls = 0;
  const u64 old = __hip_atomic_load(c.cw + CW_EPOCH, RLX_AGENT);
  const bool isS = label < 2, isO = (label == 2 || label == 3) && rank < 14;
  const int sid = rank * 2 + label, oid = rank * 2 + (label - 2);
  const bool docal = label == 3 && rank == 31;
  unsigned best[4] = {1u << 30, 1u << 30, 1u << 30, 1u << 30};
  if (w == 7) {
    if (docal) { if (lane < 16) st32s((unsigned)lane, c.rs, (int)XCH_OFF + (24 + (lane >> 2)) * 32768 + (lane & 3) * 8192); }
    if (isS) {
      const int rec = sid * 512;
#pragma unroll
      for (int k = 0; k < 8; ++k)
        if (lane < 28) st128s(v4u{SENT, SENT, SENT, SENT}, c.rs, xch_q(k >> 2, 1 + (rec >> 13), k & 3) + (rec & 8191) + lane * 16);
    } else if (isO) {
      const int oc = oid, oks = oc >> 1, oksq = oks >= 7 ? oks - 7 : oks, ohf = oks >= 7 ? 1 : 0;
      const int pos = oksq * 1024 + ((2 * (oc & 1) + (lane >> 5)) * 16 + ohf * 8 + ((lane >> 2) & 7)) * 16 + (lane & 3) * 4;
#pragma unroll
      for (int k = 0; k < 8; ++k) st32s(SENT, c.rs, xch_q(1 + (k >> 2), 0, k & 3) + pos);
    }
  }
  {
    const int m = label * 4 + (rank >> 3), blk = rank & 7;
    if (m < 16) prep_block<true>(p, c, lds, m, blk >> 1, blk & 1, docal, best);
    else prep_block<false>(p, c, lds, m, blk >> 1, blk & 1, docal, best);
    const unsigned lo = __builtin_amdgcn_readfirstlane((unsigned)old), hi = __builtin_amdgcn_readfirstlane((unsigned)(old >> 32));
    c.tagw = ((((u64)hi << 32) | lo) + 1) << 8;
    vdrain();
    lbar();
    if (tid == 0) cw_set(c, CW_PF + m * 8 + blk, 1u);
    if (docal && tid == 448) {
      const unsigned m01 = best[1] < best[0] ? best[1] : best[0], m23 = best[3] < best[2] ? best[3] : best[2];
      const unsigned q = m23 < m01 ? (best[3] < best[2] ? 3u : 2u) : (best[1] < best[0] ? 1u : 0u);
      cw_set(c, CW_CLASS, 1u | q << 1);
    }
  }
  if (isS) role_S(p, c, lds, sid >> 2, sid & 3);
  else if (isO) role_O(p, c, lds, oid);
  else if (label < 4) {}
  else if (rank < 16) role_KV(p, c, lds, rank * 4 + (label - 4));
  else role_W(p, c, lds, (rank - 16) * 4 + (label - 4));
}

static unsigned char* align_ws(void* d_ws) { return (unsigned char*)(((uintptr_t)d_ws + (2u << 20) - 1) & ~(uintptr_t)((2u << 20) - 1)); }
static void launch_v25(void* const* d_in, void* d_out, void* d_ws, hipStream_t stream) {
  Params p;
  p.x = (const u16*)d_in[0]; p.mem0 = (const u16*)d_in[1]; p.Wq = (const u16*)d_in[2]; p.Wk = (const u16*)d_in[3]; p.Wv = (const u16*)d_in[4]; p.Wo = (const u16*)d_in[5];
  p.bx = (const u16*)d_in[7]; p.wqkv = (const u16*)d_in[8]; p.wo = (const u16*)d_in[9];
  p.out = (u16*)d_out; p.ws = align_ws(d_ws);
  (void)hipFuncSetAttribute((const void*)k_main, hipFuncAttributeMaxDynamicSharedMemorySize, 128 * 1024);
  k_main<<<256, 512, 128 * 1024, stream>>>(p);
}
extern "C" void kernel_launch(void* const* d_in, const int* in_sizes, int n_in, void* d_out, int out_size, void* d_ws, size_t ws_size,
                              hipStream_t stream) {
  (void)in_sizes; (void)n_in; (void)out_size; (void)ws_size;
  launch_v25(d_in, d_out, d_ws, stream);
}
```

```cpp
#include <hip/hip_runtime.h>
#include <hip/hip_bf16.h>
#include <stdint.h>

typedef unsigned short u16;
typedef unsigned long long u64;
typedef unsigned v4u __attribute__((ext_vector_type(4)));
typedef unsigned v2u __attribute__((ext_vector_type(2)));
typedef float f32x4 __attribute__((ext_vector_type(4)));
typedef float f32x2 __attribute__((ext_vector_type(2)));
typedef short bf16x8 __attribute__((ext_vector_type(8)));
typedef __bf16 bf16x2_t __attribute__((ext_vector_type(2)));
#define GAS __attribute__((address_space(1)))
#define LAS __attribute__((address_space(3)))
#define DI __device__ __forceinline__
#define RLX_AGENT __ATOMIC_RELAXED, __HIP_MEMORY_SCOPE_AGENT

constexpr int D = 448, H = 16, E = 256, NSEG = 64;
constexpr unsigned SENT = 0xFFFFFFFFu;
constexpr int AUX_SC1 = 16;
constexpr float LOG2E = 1.4426950408889634f;
constexpr unsigned SPIN_CAP = 1u << 20;
#define DMAW 7
#define NAP_OM 7

constexpr size_t XCH_OFF = 0;
constexpr size_t CTRL_OFF = 1u << 20;
constexpr size_t WF_OFF = 2u << 20;
constexpr size_t KKF_OFF = 16u << 20;
constexpr size_t VVF_OFF = 46u << 20;
constexpr size_t KKM_OFF = 76u << 20;
constexpr size_t VMF_OFF = 84u << 20;
constexpr size_t SXX_OFF = 92u << 20;
constexpr size_t KVP_OFF = 97u << 20;
constexpr size_t WOF_OFF = 98u << 20;
constexpr size_t PQ_OFF = 100u << 20;
constexpr size_t WS_USED = 128u << 20;
constexpr int CW_EPOCH = 0;
constexpr int CW_CLASS = 16;
constexpr int CW_TMO = 32;
constexpr int CW_PF = 64;
constexpr int CW_B1 = CW_PF + 256 * 16;
constexpr int CW_PROG = CW_B1 + 64;
constexpr int CW_PM = CW_PROG + 64 * 16;

DI unsigned pk2(float a, float b) { f32x2 v = {a, b}; bf16x2_t r = __builtin_convertvector(v, bf16x2_t); return __builtin_bit_cast(unsigned, r); }
DI f32x4 mfma(v4u a, v4u b, f32x4 c) { return __builtin_amdgcn_mfma_f32_16x16x32_bf16(__builtin_bit_cast(bf16x8, a), __builtin_bit_cast(bf16x8, b), c, 0, 0, 0); }
DI v4u ld128s(__amdgpu_buffer_rsrc_t rs, int off) { return __builtin_amdgcn_raw_buffer_load_b128(rs, off, 0, AUX_SC1); }
DI v2u ld64s(__amdgpu_buffer_rsrc_t rs, int off) { return __builtin_amdgcn_raw_buffer_load_b64(rs, off, 0, AUX_SC1); }
DI unsigned ld32s(__amdgpu_buffer_rsrc_t rs, int off) { return __builtin_amdgcn_raw_buffer_load_b32(rs, off, 0, AUX_SC1); }
DI void st128s(v4u v, __amdgpu_buffer_rsrc_t rs, int off) { __builtin_amdgcn_raw_buffer_store_b128(v, rs, off, 0, AUX_SC1); }
DI void st64s(v2u v, __amdgpu_buffer_rsrc_t rs, int off) { __builtin_amdgcn_raw_buffer_store_b64(v, rs, off, 0, AUX_SC1); }
DI void st32s(unsigned v, __amdgpu_buffer_rsrc_t rs, int off) { __builtin_amdgcn_raw_buffer_store_b32(v, rs, off, 0, AUX_SC1); }
DI unsigned canon(unsigned x) { return x < 0xFFFFFFFEu ? x : 0xFFFFFFFEu; }
DI unsigned umax(unsigned a, unsigned b) { return a > b ? a : b; }
DI unsigned mx4(v4u v) { return umax(umax(v.x, v.y), umax(v.z, v.w)); }
DI bool ok4(v4u v) { return mx4(v) != SENT; }
DI void lbar() { asm volatile("s_waitcnt lgkmcnt(0)\n\ts_barrier" ::: "memory"); }
DI void vdrain() { asm volatile("s_waitcnt vmcnt(0)" ::: "memory"); }
#define MEMBAR() asm volatile("" ::: "memory")
#define BLD(a, off, rs) asm volatile("buffer_load_dwordx4 %0, %1, %2, 0 offen sc1" : "+v"(a) : "v"(off), "s"(rs) : "memory")
#define BWAIT(n, a) asm volatile("s_waitcnt vmcnt(" #n ")" : "+v"(a) :: "memory")
DI float vmax(float a, float b) { return __builtin_elementwise_maximum(a, b); }
DI float swap32_add(float x) { auto r = __builtin_amdgcn_permlane32_swap(__float_as_uint(x), __float_as_uint(x), false, false); return __uint_as_float(r[0]) + __uint_as_float(r[1]); }
DI float swap16_add(float x) { auto r = __builtin_amdgcn_permlane16_swap(__float_as_uint(x), __float_as_uint(x), false, false); return __uint_as_float(r[0]) + __uint_as_float(r[1]); }
DI float swap32_max(float x) { auto r = __builtin_amdgcn_permlane32_swap(__float_as_uint(x), __float_as_uint(x), false, false); return vmax(__uint_as_float(r[0]), __uint_as_float(r[1])); }
DI float swap16_max(float x) { auto r = __builtin_amdgcn_permlane16_swap(__float_as_uint(x), __float_as_uint(x), false, false); return vmax(__uint_as_float(r[0]), __uint_as_float(r[1])); }
DI float ror8(float x) { return __uint_as_float((unsigned)__builtin_amdgcn_update_dpp(0, (int)__float_as_uint(x), 0x128, 0xf, 0xf, false)); }
DI float ex2(float x) { return __builtin_amdgcn_exp2f(x); }
DI float bflo(unsigned u) { return __uint_as_float(u << 16); }
DI float bfhi(unsigned u) { return __uint_as_float(u & 0xffff0000u); }
DI int xch_q(int slot, int blk, int q) { return (slot * 5 + blk) * 32768 + q * 8192; }
DI int xch_off(int slot, int blk, int cls) { return xch_q(slot, blk, blk == 0 ? cls ^ 2 : cls); }

struct Params {
  const u16* x; const u16* mem0; const u16* Wq; const u16* Wk; const u16* Wv; const u16* Wo;
  const u16* bx; const u16* wqkv; const u16* wo;
  u16* out; unsigned char* ws;
};
struct Ctx { __amdgpu_buffer_rsrc_t rs; GAS u64* cw; u64 tagw; int cls; };

DI void cw_set(const Ctx& c, int idx, unsigned n) { __hip_atomic_store(c.cw + idx, c.tagw | n, RLX_AGENT); }
DI bool cw_ge(const Ctx& c, u64 v, unsigned want) { const u64 d = v - c.tagw; return d <= 255ull && (unsigned)d >= want; }
DI bool cw_tmo(const Ctx& c) { return __builtin_amdgcn_readfirstlane((int)cw_ge(c, __hip_atomic_load(c.cw + CW_TMO, RLX_AGENT), 1u)) != 0; }
DI bool wait_vec(const Ctx& c, int idx, unsigned want, int nap) {
  for (unsigned spins = 0;; ++spins) {
    const u64 v = __hip_atomic_load(c.cw + idx, RLX_AGENT);
    if (__all(cw_ge(c, v, want))) return true;
    if (spins > SPIN_CAP) { cw_set(c, CW_TMO, 1u); return false; }
    if ((spins & 255u) == 255u && cw_tmo(c)) return false;
    if (nap == 1) __builtin_amdgcn_s_sleep(1); else __builtin_amdgcn_s_sleep(4);
  }
}
DI bool wait_prog(const Ctx& c, int idx, unsigned want, unsigned& seen) {
  if (seen >= want) return true;
  const bool ok = wait_vec(c, idx, want, 1);
  unsigned v = (unsigned)(__hip_atomic_load(c.cw + idx, RLX_AGENT) - c.tagw) & 255u;
#pragma unroll
  for (int d = 1; d < 64; d <<= 1) { const unsigned o = (unsigned)__shfl_xor((int)v, d); v = o < v ? o : v; }
  seen = ok ? (unsigned)__builtin_amdgcn_readfirstlane((int)v) : want;
  return ok;
}
DI bool poll128(const Ctx& c, int off, v4u& m) {
  unsigned spins = 0;
  bool good = true;
  do {
    asm volatile("" ::: "memory");
    m = ld128s(c.rs, off);
    if (++spins > SPIN_CAP) { good = false; break; }
  } while (!ok4(m));
  if (!__all(good)) { cw_set(c, CW_TMO, 1u); return false; }
  return true;
}

template <bool ISA>
DI void prep_block(const Params& p, const Ctx& c, unsigned char* lds, int m, int ra, int cb, bool docal, unsigned (&best)[4]) {
  const int tid = threadIdx.x, lane = tid & 63, w = __builtin_amdgcn_readfirstlane(tid >> 6);
  const int h = m & 15, r16 = lane & 15, gq = lane >> 4;
  constexpr int RS = 272, L_R = 0, L_L = 224 * RS;
  const u16* rowsrc = ISA ? p.Wk : p.Wv;
  v4u gr[2][7], gl[2][4];
#pragma unroll
  for (int hf = 0; hf < 2; ++hf) {
#pragma unroll
    for (int i = 0; i < 7; ++i) { const int q = tid + 512 * i; gr[hf][i] = *(const v4u*)(rowsrc + ((size_t)(224 * cb + (q >> 4)) * H + h) * E + 128 * hf + (q & 15) * 8); }
#pragma unroll
    for (int i = 0; i < 4; ++i) {
      const int q = (i < 3 || tid < 256) ? tid + 512 * i : tid;
      if constexpr (ISA) gl[hf][i] = *(const v4u*)(p.Wq + ((size_t)(112 * ra + (q >> 4)) * H + h) * E + 128 * hf + (q & 15) * 8);
      else { const int e = q / 14, ch = q - e * 14; gl[hf][i] = *(const v4u*)(p.Wo + ((size_t)h * E + 128 * hf + e) * D + 112 * ra + ch * 8); }
    }
  }
  f32x4 acc[7][2];
#pragma unroll
  for (int t = 0; t < 7; ++t) { acc[t][0] = f32x4{0, 0, 0, 0}; acc[t][1] = f32x4{0, 0, 0, 0}; }
#pragma unroll
  for (int hf = 0; hf < 2; ++hf) {
    if (hf) lbar();
#pragma unroll
    for (int i = 0; i < 7; ++i) { const int q = tid + 512 * i; *(v4u*)(lds + L_R + (q >> 4) * RS + (q & 15) * 16) = gr[hf][i]; }
#pragma unroll
    for (int i = 0; i < 4; ++i) {
      const int q = (i < 3 || tid < 256) ? tid + 512 * i : tid;
      if constexpr (ISA) *(v4u*)(lds + L_L + (q >> 4) * RS + (q & 15) * 16) = gl[hf][i];
      else { const int e = q / 14, ch = q - e * 14; *(v4u*)(lds + L_L + e * 224 + ch * 16) = gl[hf][i]; }
    }
    lbar();
    if (w < 7) {
      v4u fa[2][2], fb[2][7];
      auto rda = [&](int es, int j) { return *(const v4u*)(lds + L_R + (32 * w + 16 * j + r16) * RS + es * 64 + gq * 16); };
      auto rdb = [&](int es, int t) {
        if constexpr (ISA) return *(const v4u*)(lds + L_L + (16 * t + r16) * RS + es * 64 + gq * 16);
        else {
          const int q = (lane & 15) >> 2, pp = lane & 3;
          const int base = L_L + (32 * es + 8 * gq + q) * 224 + (16 * t + 4 * pp) * 2;
          typedef short v4s __attribute__((ext_vector_type(4)));
          v4s lo = __builtin_amdgcn_ds_read_tr16_b64_v4i16((LAS v4s*)((LAS unsigned char*)lds + base));
          v4s hi = __builtin_amdgcn_ds_read_tr16_b64_v4i16((LAS v4s*)((LAS unsigned char*)lds + base + 4 * 224));
          v2u l2 = __builtin_bit_cast(v2u, lo), h2 = __builtin_bit_cast(v2u, hi);
          return v4u{l2.x, l2.y, h2.x, h2.y};
        }
      };
      auto rd = [&](int es, int bf) {
        fa[bf][0] = rda(es, 0); fa[bf][1] = rda(es, 1);
#pragma unroll
        for (int t = 0; t < 7; ++t) fb[bf][t] = rdb(es, t);
      };
      auto put = [&](int t, float sc, int gq2) {
        unsigned x0 = pk2(acc[t][0][0] * sc, acc[t][0][1] * sc), x1 = pk2(acc[t][0][2] * sc, acc[t][0][3] * sc);
        unsigned y0 = pk2(acc[t][1][0] * sc, acc[t][1][1] * sc), y1 = pk2(acc[t][1][2] * sc, acc[t][1][3] * sc);
        auto s0 = __builtin_amdgcn_permlane16_swap(x0, y0, false, false);
        auto s1 = __builtin_amdgcn_permlane16_swap(x1, y1, false, false);
        st128s(v4u{s0[0], s1[0], s0[1], s1[1]}, c.rs, (int)(WF_OFF + ((size_t)((m * 28 + 7 * ra + t) * 14 + 7 * cb + w) * 64 + r16 + 16 * gq2) * 16));
      };
      if (hf == 0) {
        rd(0, 0);
#pragma unroll
        for (int es = 0; es < 4; ++es) {
          __builtin_amdgcn_sched_barrier(0);
          if (es < 3) rd(es + 1, (es + 1) & 1);
          __builtin_amdgcn_sched_barrier(0);
#pragma unroll
          for (int t = 0; t < 7; ++t) { acc[t][0] = mfma(fa[es & 1][0], fb[es & 1][t], acc[t][0]); acc[t][1] = mfma(fa[es & 1][1], fb[es & 1][t], acc[t][1]); }
        }
        __builtin_amdgcn_sched_barrier(0);
      } else {
        v4u ga[4][2], gb[2][4];
#pragma unroll
        for (int es = 0; es < 4; ++es) { ga[es][0] = rda(es, 0); ga[es][1] = rda(es, 1); gb[0][es] = rdb(es, 0); }
        const float sc = ISA ? 0.0625f : 1.0f;
        const int gq2 = (gq >> 1) + 2 * (gq & 1);
#pragma unroll
        for (int t = 0; t < 7; ++t) {
          __builtin_amdgcn_sched_barrier(0);
          if (t < 6) {
#pragma unroll
            for (int es = 0; es < 4; ++es) gb[(t + 1) & 1][es] = rdb(es, t + 1);
          }
          __builtin_amdgcn_sched_barrier(0);
#pragma unroll
          for (int es = 0; es < 4; ++es) { acc[t][0] = mfma(ga[es][0], gb[t & 1][es], acc[t][0]); acc[t][1] = mfma(ga[es][1], gb[t & 1][es], acc[t][1]); }
          if (t > 0) put(t - 1, sc, gq2);
        }
        __builtin_amdgcn_sched_barrier(0);
        put(6, sc, gq2);
      }
    } else if (docal) {
#pragma unroll 1
      for (int rep = 0; rep < 8; ++rep) {
        const int q = rep & 3;
        vdrain();
        const unsigned long long t0 = __builtin_amdgcn_s_memtime();
        const unsigned x = ld32s(c.rs, (int)XCH_OFF + (24 + 2 * hf + (rep >> 2)) * 32768 + q * 8192);
        asm volatile("s_waitcnt vmcnt(0)" :: "v"(x) : "memory");
        const unsigned d = (unsigned)(__builtin_amdgcn_s_memtime() - t0);
        if (q == 0) best[0] = d < best[0] ? d : best[0]; else if (q == 1) best[1] = d < best[1] ? d : best[1]; else if (q == 2) best[2] = d < best[2] ? d : best[2]; else best[3] = d < best[3] ? d : best[3];
      }
    }
  }
}
DI v4u ld_wf(const Ctx& c, int tile, int ks, int lane) { return ld128s(c.rs, (int)(WF_OFF + ((size_t)(tile * 14 + ks) * 64 + lane) * 16)); }
DI v4u ld_mem0(const Params& p, int k, int lane) { return *(const v4u*)(p.mem0 + (lane & 7) * D + 32 * (k + 7 * ((lane >> 3) & 1)) + 8 * (lane >> 4)); }
constexpr int L_CLS = 15 * 1024;
DI bool wait_ring(Ctx& c, unsigned char* lds, int lane, bool needS) {
  bool ok = true;
  if (needS) ok = wait_vec(c, CW_PF + lane * 16, 1u, 1);
  const int l = lane < 28 ? lane : 0;
  ok = wait_vec(c, CW_PF + (64 + (l < 14 ? l : l + 18)) * 16, 1u, 1) && ok;
  ok = wait_vec(c, CW_CLASS, 1u, 1) && ok;
  const unsigned cls = (unsigned)(__hip_atomic_load(c.cw + CW_CLASS, RLX_AGENT) >> 1) & 3u;
  if (lane == 0) *(unsigned*)(lds + L_CLS) = cls;
  return ok;
}
DI void get_class(Ctx& c, const unsigned char* lds) { c.cls = __builtin_amdgcn_readfirstlane(*(const int*)(lds + L_CLS)); }

constexpr int L_MEM = 0;
constexpr int L_C = 8192;
constexpr int L_VX = 16384;
constexpr int L_P = 8192;

DI void role_S(const Params& p, Ctx& c, unsigned char* lds, int h, int g) {
  const int tid = threadIdx.x, lane = tid & 63, w = __builtin_amdgcn_readfirstlane(tid >> 6);
  const int gq = lane >> 4, c16 = lane & 15;
  const int rec = (h * 4 + g) * 512;
  const int recblk = 1 + (rec >> 13), recin = rec & 8191;
  bool alive = true;
  if (w < 4) {
    alive = wait_vec(c, CW_PF + (h * 8 + 2 * g + (lane & 1)) * 16, 1u, 1);
    v4u wa[2][14];
#pragma unroll
    for (int t2 = 0; t2 < 2; ++t2)
#pragma unroll
      for (int ks = 0; ks < 14; ++ks) {
        const int tile = (w == 3 && t2 == 1) ? 6 : 2 * w + t2;
        wa[t2][ks] = ld_wf(c, h * 28 + 7 * g + tile, ks, lane);
      }
    int maddr[2];
#pragma unroll
    for (int t2 = 0; t2 < 2; ++t2) {
      const int tile = (w == 3 && t2 == 1) ? 6 : 2 * w + t2;
      const int a = 112 * g + 16 * tile + 4 * gq, ks = a >> 5, ksq = ks >= 7 ? ks - 7 : ks, hf = ks >= 7 ? 1 : 0;
      maddr[t2] = L_MEM + ksq * 1024 + (((a & 31) >> 3) * 16 + hf * 8 + (c16 & 7)) * 16 + (a & 7) * 2;
    }
    *(v4u*)(lds + L_MEM + w * 1024 + lane * 16) = ld_mem0(p, w, lane);
    for (int s = 0; s < NSEG; ++s) {
      if (s > 0) {
        v4u m;
        alive = poll128(c, xch_off(s & 3, 0, c.cls) + w * 1024 + lane * 16, m) && alive;
        *(v4u*)(lds + L_MEM + w * 1024 + lane * 16) = m;
      }
      lbar();
      if (s == 0) { get_class(c, lds); }
      v4u mf[7];
#pragma unroll
      for (int k = 0; k < 7; ++k) mf[k] = *(const v4u*)(lds + L_MEM + k * 1024 + lane * 16);
      const v2u mb0 = *(const v2u*)(lds + maddr[0]), mb1 = *(const v2u*)(lds + maddr[1]);
      float v0[4], v1[4];
      f32x4 d2;
      if (w < 3) {
        f32x4 a1 = {0, 0, 0, 0}, a2 = {0, 0, 0, 0}, b1 = {0, 0, 0, 0}, b2 = {0, 0, 0, 0};
#pragma unroll
        for (int k = 0; k < 7; ++k) {
          a1 = mfma(wa[0][k], mf[k], a1); a2 = mfma(wa[0][k + 7], mf[k], a2);
          b1 = mfma(wa[1][k], mf[k], b1); b2 = mfma(wa[1][k + 7], mf[k], b2);
        }
#pragma unroll
        for (int r = 0; r < 4; ++r) { v0[r] = (lane & 8) ? a2[r] : a1[r]; v1[r] = (lane & 8) ? b2[r] : b1[r]; }
        d2 = mfma(v4u{pk2(v0[0], v0[1]), pk2(v0[2], v0[3]), pk2(v1[0], v1[1]), pk2(v1[2], v1[3])}, v4u{mb0.x, mb0.y, mb1.x, mb1.y}, f32x4{0, 0, 0, 0});
      } else {
        f32x4 a1 = {0, 0, 0, 0}, a2 = {0, 0, 0, 0};
#pragma unroll
        for (int k = 0; k < 7; ++k) { a1 = mfma(wa[0][k], mf[k], a1); a2 = mfma(wa[0][k + 7], mf[k], a2); }
#pragma unroll
        for (int r = 0; r < 4; ++r) { v0[r] = (lane & 8) ? a2[r] : a1[r]; v1[r] = 0.f; }
        d2 = mfma(v4u{pk2(v0[0], v0[1]), pk2(v0[2], v0[3]), 0u, 0u}, v4u{mb0.x, mb0.y, 0u, 0u}, f32x4{0, 0, 0, 0});
      }
      {
        f32x4 o;
#pragma unroll
        for (int r = 0; r < 4; ++r) o[r] = swap32_add(d2[r]);
        if (c16 < 8 && gq < 2) *(f32x4*)(lds + L_P + ((w * 2 + gq) * 8 + c16) * 16) = o;
      }
      lbar();
      {
        const int cb = (s * 16 + h) * 56 + 14 * g + 4 * w + (gq >> 1);
        float f[4];
#pragma unroll
        for (int r = 0; r < 4; ++r) f[r] = v0[r] + ror8(v0[r]);
        if (c16 < 8) st64s(v2u{pk2(f[0], f[1]), pk2(f[2], f[3])}, c.rs, (int)(KKM_OFF + (size_t)cb * 128 + c16 * 16 + (gq & 1) * 8));
        if (w < 3) {
#pragma unroll
          for (int r = 0; r < 4; ++r) f[r] = v1[r] + ror8(v1[r]);
          if (c16 < 8) st64s(v2u{pk2(f[0], f[1]), pk2(f[2], f[3])}, c.rs, (int)(KKM_OFF + (size_t)(cb + 2) * 128 + c16 * 16 + (gq & 1) * 8));
        }
      }
      if (!alive) break;
    }
    vdrain();
    lbar();
  } else {
    unsigned kkprog = 0;
    v4u kf[7];
    auto load_kk = [&](int s) -> bool {
      if (!wait_prog(c, CW_PROG + (2 * h + (lane & 1)) * 16, s + 1, kkprog)) return false;
#pragma unroll
      for (int k = 0; k < 7; ++k) kf[k] = ld128s(c.rs, (int)(KKF_OFF + ((size_t)(((s * 16 + h) * 4 + g) * 7 + k) * 64 + lane) * 16));
      return true;
    };
    if (w < 7) *(v4u*)(lds + L_MEM + w * 1024 + lane * 16) = ld_mem0(p, w, lane);
    if (w == 6) alive = wait_ring(c, lds, lane, false);
    if (w == 7) alive = load_kk(0);
    for (int s = 0; s < NSEG; ++s) {
      const int slot = s & 3;
      if (s > 0 && w < 7) {
        v4u m;
        alive = poll128(c, xch_off(slot, 0, c.cls) + w * 1024 + lane * 16, m) && alive;
        *(v4u*)(lds + L_MEM + w * 1024 + lane * 16) = m;
      }
      lbar();
      if (s == 0) get_class(c, lds);
      if (w == 5 && s > 0 && lane == 0) cw_set(c, CW_PM + (h * 4 + g) * 16, (unsigned)s);
      if (w == 7 && s < NSEG - 1) {
        __builtin_amdgcn_s_setprio(2);
        v4u mf[7];
#pragma unroll
        for (int k = 0; k < 7; ++k) mf[k] = *(const v4u*)(lds + L_MEM + k * 1024 + lane * 16);
        __builtin_amdgcn_sched_barrier(0);
        f32x4 ax = {0, 0, 0, 0}, ay = {0, 0, 0, 0};
#pragma unroll
        for (int k = 0; k < 7; ++k) { if (k & 1) ay = mfma(kf[k], mf[k], ay); else ax = mfma(kf[k], mf[k], ax); }
        float sx[4], a[4]; unsigned t[4];
#pragma unroll
        for (int r = 0; r < 4; ++r) a[r] = ax[r] + ay[r];
        __builtin_amdgcn_sched_barrier(0);
#pragma unroll
        for (int r = 0; r < 4; ++r) t[r] = (unsigned)__builtin_amdgcn_mov_dpp((int)__float_as_uint(a[r]), 0x128, 0xf, 0xf, false);
        __builtin_amdgcn_sched_barrier(0);
#pragma unroll
        for (int r = 0; r < 4; ++r) { auto q = __builtin_amdgcn_permlane32_swap(t[r], __float_as_uint(ay[r]), false, false); t[r] = q[1]; }
        __builtin_amdgcn_sched_barrier(0);
#pragma unroll
        for (int r = 0; r < 4; ++r) sx[r] = a[r] + __uint_as_float(t[r]);
        const float m = swap16_max(vmax(vmax(sx[0], sx[1]), vmax(sx[2], sx[3])));
        const int ro = xch_off(slot, recblk, c.cls) + recin;
        const float mb = -m * LOG2E;
        float e[4];
#pragma unroll
        for (int r = 0; r < 4; ++r) e[r] = ex2(fmaf(sx[r], LOG2E, mb));
        if (c16 < 8 && gq < 2) st64s(v2u{canon(pk2(e[0], e[1])), canon(pk2(e[2], e[3]))}, c.rs, ro + 256 + c16 * 16 + gq * 8);
        const float l = swap16_add((e[0] + e[1]) + (e[2] + e[3]));
        if (c16 < 8 && gq == 0) st64s(v2u{canon(__float_as_uint(m)), canon(__float_as_uint(l))}, c.rs, ro + 384 + c16 * 8);
        __builtin_amdgcn_s_setprio(0);
      }
      lbar();
      if (w == 7 && s + 1 < NSEG - 1) alive = alive && load_kk(s + 1);
      if (w == 4 && s < NSEG - 1) {
        const int i = lane & 7, gg = (lane >> 3) & 1;
        f32x4 t0 = *(const f32x4*)(lds + L_P + ((0 + gg) * 8 + i) * 16), t1 = *(const f32x4*)(lds + L_P + ((2 + gg) * 8 + i) * 16);
        f32x4 t2 = *(const f32x4*)(lds + L_P + ((4 + gg) * 8 + i) * 16), t3 = *(const f32x4*)(lds + L_P + ((6 + gg) * 8 + i) * 16);
        f32x4 o = (t0 + t1) + (t2 + t3);
        const int ro = xch_off(slot, recblk, c.cls) + recin;
        if (lane < 16) st64s(v2u{canon(pk2(o[0], o[1])), canon(pk2(o[2], o[3]))}, c.rs, ro + i * 16 + gg * 8);
        if (lane < 28) st128s(v4u{SENT, SENT, SENT, SENT}, c.rs, xch_off((s + 2) & 3, recblk, c.cls) + recin + lane * 16);
      }
      if (!alive) break;
    }
    lbar();
    if (w == 5 && lane == 0) {
      if (alive) cw_set(c, CW_PM + (h * 4 + g) * 16, (unsigned)NSEG);
      if (h == 0 && g == 0) __hip_atomic_store(c.cw + CW_EPOCH, c.tagw >> 8, RLX_AGENT);
    }
  }
}

DI void role_O(const Params& p, Ctx& c, unsigned char* lds, int oc) {
  const int tid = threadIdx.x, lane = tid & 63, w = __builtin_amdgcn_readfirstlane(tid >> 6);
  const int gq = lane >> 4, c16 = lane & 15;
  bool alive = wait_vec(c, CW_PF + ((16 + 2 * w + (lane & 1)) * 8 + 2 * (oc / 7) + ((lane >> 1) & 1)) * 16, 1u, 1);
  v4u wb[2][14];
#pragma unroll
  for (int hh = 0; hh < 2; ++hh)
#pragma unroll
    for (int ks = 0; ks < 14; ++ks) wb[hh][ks] = ld_wf(c, 448 + (2 * w + hh) * 28 + oc, ks, lane);
  if (w < 7) *(v4u*)(lds + L_MEM + w * 1024 + lane * 16) = ld_mem0(p, w, lane);
  const int half = oc >= 14 ? 1 : 0;
  unsigned vprog = 0;
  auto load_vv = [&](int s) -> bool {
    const bool ok = wait_prog(c, CW_PROG + (32 + 2 * (lane & 15) + half) * 16, s + 1, vprog);
#pragma unroll
    for (int q = 0; q < 4; ++q) {
      const int src = (int)(VVF_OFF + ((size_t)((s * 28 + oc) * 16 + 4 * q) * 64 + lane) * 16);
      const int dst = __builtin_amdgcn_readfirstlane(L_VX + (s & 1) * 16384 + q * 4096);
      asm volatile("s_mov_b32 m0, %0\n\ts_nop 0\n\tbuffer_load_dwordx4 %1, %2, 0 offen sc1 lds\n\tbuffer_load_dwordx4 %1, %2, 0 offen offset:1024 sc1 lds\n\t"
                   "buffer_load_dwordx4 %1, %2, 0 offen offset:2048 sc1 lds\n\tbuffer_load_dwordx4 %1, %2, 0 offen offset:3072 sc1 lds"
                   :: "s"(dst), "v"(src), "s"(c.rs) : "memory", "m0");
    }
    return ok;
  };
  if (w == 6) alive = wait_ring(c, lds, lane, true) && alive;
  if (w == DMAW) { alive = load_vv(0) && alive; vdrain(); }
  const int rh = 2 * w + ((lane >> 3) & 1), ri = lane & 7;
  const int rec = (rh * 4 + gq) * 512, recblk = 1 + (rec >> 13), recin = rec & 8191;
  const int oks = oc >> 1, oksq = oks >= 7 ? oks - 7 : oks, ohf = oks >= 7 ? 1 : 0;
  v4u sel;
#pragma unroll
  for (int k = 0; k < 4; ++k) sel[k] = (c16 & 7) == 2 * k ? 0x00003F80u : ((c16 & 7) == 2 * k + 1 ? 0x3F800000u : 0u);
  const bool diag = (gq >> 1) == (c16 >> 3);
  v2u zz = {0u, 0u};
  for (int s = 0; s < NSEG; ++s) {
    const int slot = s & 3;
    if (w < 7 && s > 0) {
      v4u m;
      __builtin_amdgcn_s_sleep(NAP_OM);
      alive = alive && poll128(c, xch_off(slot, 0, c.cls) + w * 1024 + lane * 16, m);
      *(v4u*)(lds + L_MEM + w * 1024 + lane * 16) = m;
    }
    lbar();
    if (s == 0) get_class(c, lds);
    if (s > 0) st64s(zz, c.rs, (int)(VMF_OFF + ((size_t)(((s - 1) * 28 + oc) * 8 + w) * 64 + lane) * 8));
    if (w == DMAW && s + 1 < NSEG - 1) alive = load_vv(s + 1) && alive;
    v4u mf[7];
#pragma unroll
    for (int k = 0; k < 7; ++k) mf[k] = *(const v4u*)(lds + L_MEM + k * 1024 + lane * 16);
    v4u vx[2];
#pragma unroll
    for (int hh = 0; hh < 2; ++hh) vx[hh] = *(const v4u*)(lds + L_VX + (s & 1) * 16384 + (2 * w + hh) * 1024 + lane * 16);
    {
      float z[2][4];
#pragma unroll
      for (int hh = 0; hh < 2; ++hh) {
        f32x4 aA = {0, 0, 0, 0}, aB = {0, 0, 0, 0};
#pragma unroll
        for (int k = 0; k < 7; ++k) { aA = mfma(mf[k], wb[hh][k], aA); aB = mfma(mf[k], wb[hh][k + 7], aB); }
#pragma unroll
        for (int r = 0; r < 4; ++r) z[hh][r] = swap32_add(gq < 2 ? aA[r] : aB[r]);
      }
      zz = gq < 2 ? v2u{pk2(z[0][0], z[0][1]), pk2(z[0][2], z[0][3])} : v2u{pk2(z[1][0], z[1][1]), pk2(z[1][2], z[1][3])};
    }
    asm volatile("" :: "v"(zz));
    if (s < NSEG - 1) {
      const int ro = xch_off(slot, recblk, c.cls) + recin;
      v4u c1 = {SENT, SENT, SENT, SENT}, c2; v2u c3;
      for (unsigned spins = 0;; ++spins) {
        asm volatile("" ::: "memory");
        c3 = ld64s(c.rs, ro + 384 + ri * 8); c2 = ld128s(c.rs, ro + 256 + ri * 16);
        if (__all(umax(mx4(c2), umax(c3.x, c3.y)) != SENT)) break;
        if (spins > SPIN_CAP) { cw_set(c, CW_TMO, 1u); alive = false; break; }
      }
      const int so = ro + ri * 16;
      BLD(c1, so, c.rs);
      __builtin_amdgcn_sched_barrier(0);
      const float mg = __uint_as_float(c3.x), lg = __uint_as_float(c3.y);
      const float mx = swap32_max(swap16_max(mg));
      const float f = ex2((mg - mx) * LOG2E);
      float ef[8];
#pragma unroll
      for (int k = 0; k < 4; ++k) { ef[2 * k] = bflo(c2[k]); ef[2 * k + 1] = bfhi(c2[k]); }
      const float lx = swap32_add(swap16_add(f * lg));
      v4u ax;
#pragma unroll
      for (int k = 0; k < 4; ++k) ax[k] = pk2(ef[2 * k] * f, ef[2 * k + 1] * f);
      const f32x4 oa = mfma(vx[0], ax, f32x4{0, 0, 0, 0}), ob = mfma(vx[1], ax, f32x4{0, 0, 0, 0});
      f32x4 ox;
#pragma unroll
      for (int r = 0; r < 4; ++r) ox[r] = c16 < 8 ? oa[r] : ob[r];
      const float mxl = mx * LOG2E;
      asm volatile("" :: "v"(ox), "v"(lx), "v"(mxl));
#pragma nounroll
      for (unsigned spins = 0;; ++spins) {
        BLD(c1, so, c.rs);
        BWAIT(1, c1);
        if (__all(ok4(c1))) break;
        if (spins > SPIN_CAP) { cw_set(c, CW_TMO, 1u); alive = false; break; }
      }
      const f32x4 sc = mfma(sel, c1, f32x4{0, 0, 0, 0});
      const float ml = swap16_max(vmax(vmax(sc[0], sc[1]), vmax(sc[2], sc[3]))) * LOG2E;
      const float mb = -vmax(ml, mxl);
      float e[4];
#pragma unroll
      for (int r = 0; r < 4; ++r) e[r] = ex2(fmaf(sc[r], LOG2E, mb));
      const float cx = ex2(mxl + mb);
      const float l = fmaf(cx, lx, swap16_add((e[0] + e[1]) + (e[2] + e[3])));
      const float rl = __builtin_amdgcn_rcpf(l);
      const float rm = diag ? rl : 0.f, kx = cx * rl;
      f32x4 o = mfma(v4u{zz.x, zz.y, 0u, 0u}, v4u{pk2(e[0] * rm, e[1] * rm), pk2(e[2] * rm, e[3] * rm), 0u, 0u}, ox * kx);
#pragma unroll
      for (int r = 0; r < 4; ++r) o[r] += ror8(o[r]);
      if (c16 < 8) *(f32x4*)(lds + L_C + ((w * 8 + c16) * 16 + 4 * gq) * 4) = o;
      lbar();
      BWAIT(0, c1);
      if (w == 7) {
        const int i = (lane >> 2) & 7, mp = (lane >> 5) * 4 + (lane & 3);
        f32x2 t[8];
#pragma unroll
        for (int k = 0; k < 8; ++k) t[k] = *(const f32x2*)(lds + L_C + ((k * 8 + i) * 16 + 2 * mp) * 4);
        f32x2 sum = ((t[0] + t[1]) + (t[2] + t[3])) + ((t[4] + t[5]) + (t[6] + t[7]));
        const int pos = oksq * 1024 + ((2 * (oc & 1) + (lane >> 5)) * 16 + ohf * 8 + i) * 16 + (lane & 3) * 4;
        st32s(canon(pk2(sum[0], sum[1])), c.rs, xch_off((s + 1) & 3, 0, c.cls) + pos);
        st32s(SENT, c.rs, xch_off((s + 3) & 3, 0, c.cls) + pos);
        if (s > 0 && lane == 0) cw_set(c, CW_PM + (64 + oc) * 16, (unsigned)s);
      }
    }
    if (!alive) break;
  }
  st64s(zz, c.rs, (int)(VMF_OFF + ((size_t)(((NSEG - 1) * 28 + oc) * 8 + w) * 64 + lane) * 8));
  vdrain();
  lbar();
  if (w == 7 && alive && lane == 0) cw_set(c, CW_PM + (64 + oc) * 16, (unsigned)NSEG);
}

constexpr int XRS = 912, XB = 32 * XRS;
DI int xrow_addr(int q) { return (q / 56) * XRS + (q % 56) * 16; }
DI void role_KV(const Params& p, Ctx& c, unsigned char* lds, int hm2) {
  const int tid = threadIdx.x, lane = tid & 63, w = __builtin_amdgcn_readfirstlane(tid >> 6);
  const int gq = lane >> 4, c16 = lane & 15;
  const int m = hm2 >> 1, half = hm2 & 1;
  const bool iskk = m < 16;
  const int h = m & 15;
  constexpr int SPB = 28 * 1024, L_SP = 2 * XB;
  constexpr int EARLY = 2;
  if (w == 7) {
    auto reduce = [&](int s) {
      const unsigned char* sp = lds + L_SP + (s & 1) * SPB + lane * 16;
#pragma unroll
      for (int qt = 0; qt < 2; ++qt) {
        f32x4 a0 = *(const f32x4*)(sp + qt * 1024), a1 = *(const f32x4*)(sp + (2 + qt) * 1024);
#pragma unroll
        for (int w2 = 1; w2 < 7; ++w2) { a0 += *(const f32x4*)(sp + (w2 * 4 + qt) * 1024); a1 += *(const f32x4*)(sp + (w2 * 4 + 2 + qt) * 1024); }
        st128s(v4u{pk2(a0[0], a0[1]), pk2(a0[2], a0[3]), pk2(a1[0], a1[1]), pk2(a1[2], a1[3])}, c.rs, (int)(SXX_OFF + ((size_t)(((s * 16 + h) * 2 + half) * 2 + qt) * 64 + lane) * 16));
      }
    };
    const u16* xs = p.x + lane * 8;
    v4u t[28];
#pragma unroll
    for (int f = 0; f < 28; ++f) t[f] = *(const v4u*)(xs + f * 512);
#pragma unroll
    for (int f = 0; f < 28; ++f) *(v4u*)(lds + xrow_addr(f * 64 + lane)) = t[f];
    for (int s = 0; s < NSEG; ++s) {
      lbar();
      const int sn = s + 1 < NSEG ? s + 1 : s;
#pragma unroll
      for (int f = 0; f < 28; ++f) t[f] = *(const v4u*)(xs + (size_t)sn * (32 * D) + f * 512);
      if (iskk && s > 0) reduce(s - 1);
#pragma unroll
      for (int f = 0; f < 28; ++f) *(v4u*)(lds + ((s + 1) & 1) * XB + xrow_addr(f * 64 + lane)) = t[f];
      vdrain();
      lbar();
      if (lane == 0 && s != EARLY) cw_set(c, CW_PROG + hm2 * 16, (unsigned)(s < EARLY ? s + 1 : s));
    }
    lbar();
    if (lane == 0) cw_set(c, CW_PROG + hm2 * 16, (unsigned)NSEG);
    if (iskk) {
      reduce(NSEG - 1);
      vdrain();
      if (lane == 0) cw_set(c, CW_PROG + hm2 * 16, (unsigned)(NSEG + 2));
    }
    return;
  }
  wait_vec(c, CW_PF + (m * 8 + 4 * half + (lane & 3)) * 16, 1u, 1);
  v4u wt[2][14];
#pragma unroll
  for (int t2 = 0; t2 < 2; ++t2)
#pragma unroll
    for (int ks = 0; ks < 14; ++ks) wt[t2][ks] = ld_wf(c, m * 28 + 14 * half + 2 * w + t2, ks, lane);
  for (int s = 0; s < NSEG; ++s) {
    lbar();
    const unsigned char* xb = lds + (s & 1) * XB;
    const unsigned char* xf0 = xb + c16 * XRS + gq * 16;
    f32x4 acc[2][2];
#pragma unroll
    for (int a = 0; a < 2; ++a) { acc[a][0] = f32x4{0, 0, 0, 0}; acc[a][1] = f32x4{0, 0, 0, 0}; }
    if (iskk) {
#pragma unroll
      for (int ks = 0; ks < 14; ++ks)
#pragma unroll
        for (int rt = 0; rt < 2; ++rt) {
          v4u xf = *(const v4u*)(xf0 + rt * 16 * XRS + ks * 64);
          acc[rt][0] = mfma(wt[0][ks], xf, acc[rt][0]); acc[rt][1] = mfma(wt[1][ks], xf, acc[rt][1]);
        }
      const int gq2 = (gq >> 1) + 2 * (gq & 1);
      v4u kf[2];
#pragma unroll
      for (int rt = 0; rt < 2; ++rt) {
        unsigned x0 = pk2(acc[rt][0][0], acc[rt][0][1]), x1 = pk2(acc[rt][0][2], acc[rt][0][3]);
        unsigned y0 = pk2(acc[rt][1][0], acc[rt][1][1]), y1 = pk2(acc[rt][1][2], acc[rt][1][3]);
        auto s0 = __builtin_amdgcn_permlane16_swap(x0, y0, false, false);
        auto s1 = __builtin_amdgcn_permlane16_swap(x1, y1, false, false);
        kf[rt] = v4u{s0[0], s1[0], s0[1], s1[1]};
      }
      vdrain();
#pragma unroll
      for (int rt = 0; rt < 2; ++rt) {
        const int g = c16 >> 2, rowq = (c16 & 3) + 4 * rt;
        st128s(kf[rt], c.rs, (int)(KKF_OFF + ((size_t)(((s * 16 + h) * 4 + g) * 7 + w) * 64 + rowq + 8 * half + 16 * gq2) * 16));
      }
#pragma unroll
      for (int qt = 0; qt < 2; ++qt) {
        const v4u xq = *(const v4u*)(xb + (16 * qt + c16) * XRS + (7 * half + w) * 64 + gq2 * 16);
#pragma unroll
        for (int kt = 0; kt < 2; ++kt)
          *(f32x4*)(lds + L_SP + (s & 1) * SPB + (w * 4 + kt * 2 + qt) * 1024 + lane * 16) = mfma(kf[kt], xq, f32x4{0, 0, 0, 0});
      }
    } else {
#pragma unroll
      for (int ks = 0; ks < 14; ++ks)
#pragma unroll
        for (int rt = 0; rt < 2; ++rt) {
          v4u xf = *(const v4u*)(xf0 + rt * 16 * XRS + ks * 64);
          acc[rt][0] = mfma(xf, wt[0][ks], acc[rt][0]); acc[rt][1] = mfma(xf, wt[1][ks], acc[rt][1]);
        }
      v4u o[2];
#pragma unroll
      for (int t2 = 0; t2 < 2; ++t2) o[t2] = v4u{pk2(acc[0][t2][0], acc[0][t2][1]), pk2(acc[0][t2][2], acc[0][t2][3]), pk2(acc[1][t2][0], acc[1][t2][1]), pk2(acc[1][t2][2], acc[1][t2][3])};
      vdrain();
#pragma unroll
      for (int t2 = 0; t2 < 2; ++t2) st128s(o[t2], c.rs, (int)(VVF_OFF + ((size_t)((s * 28 + 14 * half + 2 * w + t2) * 16 + h) * 64 + lane) * 16));
    }
    if (s < EARLY) vdrain();
    lbar();
  }
  vdrain();
  lbar();
}

constexpr int LT_X = 0, LT_PX = 30 * 1024, LT_PM = 62 * 1024;
DI void task_T(const Params& p, const Ctx& c, unsigned char* lds, int seg, int cg) {
  const int tid = threadIdx.x, lane = tid & 63, w = __builtin_amdgcn_readfirstlane(tid >> 6);
  const int gq = lane >> 4, c16 = lane & 15;
  const int oc = 7 * cg + (w < 7 ? w : 0);
#pragma unroll
  for (int i = 0; i < 4; ++i) {
    const int q = tid + 512 * i;
    if (q < 1792) *(v4u*)(lds + LT_X + xrow_addr(q)) = *(const v4u*)(p.x + (size_t)seg * (32 * D) + q * 8);
  }
  if (w == 7) wait_vec(c, CW_PROG + lane * 16, (unsigned)seg + (lane < 32 ? 2u : 1u), 4);
  lbar();
  v4u sx[2][2][2], vv[16];
#pragma unroll
  for (int hh = 0; hh < 2; ++hh)
#pragma unroll
    for (int hf = 0; hf < 2; ++hf)
#pragma unroll
      for (int qt = 0; qt < 2; ++qt) sx[hh][hf][qt] = ld128s(c.rs, (int)(SXX_OFF + ((size_t)(((seg * 16 + 2 * w + hh) * 2 + hf) * 2 + qt) * 64 + lane) * 16));
  if (w < 7) {
#pragma unroll
    for (int h = 0; h < 16; ++h) vv[h] = ld128s(c.rs, (int)(VVF_OFF + ((size_t)((seg * 28 + oc) * 16 + h) * 64 + lane) * 16));
  } else {
    wait_vec(c, CW_PM + (cg * 16 + (seg & 15)) * 16, seg + 1, 4);
    wait_vec(c, CW_PM + lane * 16, seg + 1, 1);
    wait_vec(c, CW_PM + (64 + (lane < 28 ? lane : 0)) * 16, seg + 1, 1);
  }
  lbar();
  v4u kf[14]; v2u vm[8];
#pragma unroll
  for (int ks = 0; ks < 14; ++ks) kf[ks] = ld128s(c.rs, (int)(KKM_OFF + (size_t)((seg * 16 + 2 * w + (c16 >> 3)) * 56 + 4 * ks + gq) * 128 + (c16 & 7) * 16));
  if (w < 7) {
#pragma unroll
    for (int pr = 0; pr < 8; ++pr) vm[pr] = ld64s(c.rs, (int)(VMF_OFF + ((size_t)((seg * 28 + oc) * 8 + pr) * 64 + lane) * 8));
  }
#pragma unroll
  for (int qt = 0; qt < 2; ++qt) {
    const unsigned char* xq = lds + LT_X + (16 * qt + c16) * XRS + gq * 16;
    f32x4 am = {0, 0, 0, 0}, am2 = {0, 0, 0, 0};
#pragma unroll
    for (int ks = 0; ks < 14; ks += 2) {
      am = mfma(kf[ks], *(const v4u*)(xq + ks * 64), am);
      am2 = mfma(kf[ks + 1], *(const v4u*)(xq + ks * 64 + 64), am2);
    }
    am += am2;
    const int q = 16 * qt + c16;
    float sc[2][8], mxh[2];
#pragma unroll
    for (int hh = 0; hh < 2; ++hh) {
      float m = (gq >> 1) == hh ? vmax(vmax(am[0], am[1]), vmax(am[2], am[3])) : -1e9f;
#pragma unroll
      for (int k = 0; k < 4; ++k) {
        const unsigned u0 = sx[hh][0][qt][k], u1 = sx[hh][1][qt][k];
        const int key = (k < 2 ? 4 * gq + 2 * k : 16 + 4 * gq + 2 * (k - 2));
        sc[hh][2 * k] = key <= q ? bflo(u0) + bflo(u1) : -1e9f;
        sc[hh][2 * k + 1] = key + 1 <= q ? bfhi(u0) + bfhi(u1) : -1e9f;
        m = vmax(m, vmax(sc[hh][2 * k], sc[hh][2 * k + 1]));
      }
      mxh[hh] = swap32_max(swap16_max(m)) * LOG2E;
    }
    float em[4], lh[2];
    const float mm = gq < 2 ? mxh[0] : mxh[1];
#pragma unroll
    for (int r = 0; r < 4; ++r) em[r] = ex2(fmaf(am[r], LOG2E, -mm));
    const float lm = (em[0] + em[1]) + (em[2] + em[3]);
#pragma unroll
    for (int hh = 0; hh < 2; ++hh) {
      float l = (gq >> 1) == hh ? lm : 0.f;
#pragma unroll
      for (int k = 0; k < 8; ++k) { sc[hh][k] = ex2(fmaf(sc[hh][k], LOG2E, -mxh[hh])); l += sc[hh][k]; }
      lh[hh] = __builtin_amdgcn_rcpf(swap32_add(swap16_add(l)));
      *(v4u*)(lds + LT_PX + ((2 * w + hh) * 2 + qt) * 1024 + lane * 16) =
          v4u{pk2(sc[hh][0] * lh[hh], sc[hh][1] * lh[hh]), pk2(sc[hh][2] * lh[hh], sc[hh][3] * lh[hh]), pk2(sc[hh][4] * lh[hh], sc[hh][5] * lh[hh]), pk2(sc[hh][6] * lh[hh], sc[hh][7] * lh[hh])};
    }
    const float rm = gq < 2 ? lh[0] : lh[1];
    *(v2u*)(lds + LT_PM + (w * 2 + qt) * 512 + lane * 8) = v2u{pk2(em[0] * rm, em[1] * rm), pk2(em[2] * rm, em[3] * rm)};
  }
  lbar();
  if (w < 7) {
    f32x4 o[2] = {f32x4{0, 0, 0, 0}, f32x4{0, 0, 0, 0}};
#pragma unroll
    for (int h = 0; h < 16; ++h)
#pragma unroll
      for (int qt = 0; qt < 2; ++qt) o[qt] = mfma(vv[h], *(const v4u*)(lds + LT_PX + (h * 2 + qt) * 1024 + lane * 16), o[qt]);
#pragma unroll
    for (int i = 0; i < 4; ++i)
#pragma unroll
      for (int qt = 0; qt < 2; ++qt) {
        const v2u b0 = *(const v2u*)(lds + LT_PM + ((2 * i) * 2 + qt) * 512 + lane * 8), b1 = *(const v2u*)(lds + LT_PM + ((2 * i + 1) * 2 + qt) * 512 + lane * 8);
        o[qt] = mfma(v4u{vm[2 * i].x, vm[2 * i].y, vm[2 * i + 1].x, vm[2 * i + 1].y}, v4u{b0.x, b0.y, b1.x, b1.y}, o[qt]);
      }
#pragma unroll
    for (int qt = 0; qt < 2; ++qt)
      *(v2u*)(p.out + (size_t)(32 * seg + 16 * qt + c16) * (2 * D) + 16 * oc + 4 * gq) = v2u{pk2(o[qt][0], o[qt][1]), pk2(o[qt][2], o[qt][3])};
  }
  lbar();
}

DI unsigned phi2(float a, float b) {
  const unsigned t = pk2(a, b);
  const float x = bflo(t), y = bfhi(t);
  const unsigned u = pk2(x > 0.f ? x + 1.f : __expf(x), y > 0.f ? y + 1.f : __expf(y));
  return pk2(bflo(u) + 1e-6f, bfhi(u) + 1e-6f);
}
constexpr int LB_W = 0, LB_R = 448 * 192;
DI v4u wfrag(const unsigned char* lds, int off) {
  typedef short v4s __attribute__((ext_vector_type(4)));
  v4s lo = __builtin_amdgcn_ds_read_tr16_b64_v4i16((LAS v4s*)((LAS unsigned char*)lds + off));
  v4s hi = __builtin_amdgcn_ds_read_tr16_b64_v4i16((LAS v4s*)((LAS unsigned char*)lds + off + 4 * 192));
  v2u l2 = __builtin_bit_cast(v2u, lo), h2 = __builtin_bit_cast(v2u, hi);
  return v4u{l2.x, l2.y, h2.x, h2.y};
}
DI void task_B1(const Params& p, const Ctx& c, unsigned char* lds, int h, int tq) {
  const int tid = threadIdx.x, lane = tid & 63, w = __builtin_amdgcn_readfirstlane(tid >> 6);
  const int gq = lane >> 4, c16 = lane & 15;
#pragma unroll 7
  for (int i = 0; i < 21; ++i) {
    const int idx = tid + 512 * i, k = idx / 24, rem = idx - k * 24, m = rem >> 3, sl = rem & 7;
    v2u v = {0u, 0u};
    if (sl < 7) v = *(const v2u*)(p.wqkv + (size_t)k * (3 * D) + m * D + h * 28 + sl * 4);
    *(v2u*)(lds + LB_W + k * 192 + m * 64 + sl * 8) = v;
  }
  if (w < 7) {
    const int nt = 7 * tq + w;
    unsigned e8[8];
#pragma unroll
    for (int i = 0; i < 8; ++i) { const int e = (i < 4 ? 4 * gq + i : 12 + 4 * gq + i); e8[i] = e < 28 ? (unsigned)p.wo[(size_t)(28 * h + e) * D + 16 * nt + c16] : 0u; }
    st128s(v4u{e8[0] | e8[1] << 16, e8[2] | e8[3] << 16, e8[4] | e8[5] << 16, e8[6] | e8[7] << 16}, c.rs, (int)(WOF_OFF + ((size_t)(h * 28 + nt) * 64 + lane) * 16));
  }
  lbar();
  const int t0 = 512 * tq + 64 * w;
  const u16* xr = p.bx + (size_t)(t0 + c16) * D + 8 * gq;
  const int wb = LB_W + (8 * gq + (c16 >> 2)) * 192 + (lane & 3) * 8;
  f32x4 aq[4][2], ak[4][2], av[4][2];
#pragma unroll
  for (int rt = 0; rt < 4; ++rt)
#pragma unroll
    for (int dt = 0; dt < 2; ++dt) { aq[rt][dt] = f32x4{0, 0, 0, 0}; ak[rt][dt] = f32x4{0, 0, 0, 0}; av[rt][dt] = f32x4{0, 0, 0, 0}; }
  v4u xn[4];
#pragma unroll
  for (int rt = 0; rt < 4; ++rt) xn[rt] = *(const v4u*)(xr + (size_t)(16 * rt) * D);
#pragma unroll 1
  for (int ks = 0; ks < 14; ++ks) {
    v4u x[4];
#pragma unroll
    for (int rt = 0; rt < 4; ++rt) x[rt] = xn[rt];
    const int kn = ks + 1 < 14 ? ks + 1 : ks;
#pragma unroll
    for (int rt = 0; rt < 4; ++rt) xn[rt] = *(const v4u*)(xr + (size_t)(16 * rt) * D + 32 * kn);
#pragma unroll
    for (int dt = 0; dt < 2; ++dt) {
      const v4u wq = wfrag(lds, wb + ks * 32 * 192 + dt * 32), wk = wfrag(lds, wb + ks * 32 * 192 + 64 + dt * 32), wv = wfrag(lds, wb + ks * 32 * 192 + 128 + dt * 32);
#pragma unroll
      for (int rt = 0; rt < 4; ++rt) { aq[rt][dt] = mfma(wq, x[rt], aq[rt][dt]); ak[rt][dt] = mfma(x[rt], wk, ak[rt][dt]); av[rt][dt] = mfma(x[rt], wv, av[rt][dt]); }
    }
  }
#pragma unroll
  for (int rt = 0; rt < 4; ++rt)
    st128s(v4u{phi2(aq[rt][0][0], aq[rt][0][1]), phi2(aq[rt][0][2], aq[rt][0][3]), phi2(aq[rt][1][0], aq[rt][1][1]), phi2(aq[rt][1][2], aq[rt][1][3])}, c.rs,
           (int)(PQ_OFF + ((size_t)((t0 / 16 + rt) * 16 + h) * 64 + lane) * 16));
  f32x4 kv[2][2] = {{f32x4{0, 0, 0, 0}, f32x4{0, 0, 0, 0}}, {f32x4{0, 0, 0, 0}, f32x4{0, 0, 0, 0}}};
#pragma unroll
  for (int rp = 0; rp < 2; ++rp) {
    v4u pk[2], vf[2];
#pragma unroll
    for (int dt = 0; dt < 2; ++dt) {
      const f32x4 k0 = ak[2 * rp][dt], k1 = ak[2 * rp + 1][dt], v0 = av[2 * rp][dt], v1 = av[2 * rp + 1][dt];
      pk[dt] = v4u{phi2(k0[0], k0[1]), phi2(k0[2], k0[3]), phi2(k1[0], k1[1]), phi2(k1[2], k1[3])};
      vf[dt] = v4u{pk2(v0[0], v0[1]), pk2(v0[2], v0[3]), pk2(v1[0], v1[1]), pk2(v1[2], v1[3])};
    }
    if (c16 >= 12) pk[1] = v4u{0u, 0u, 0u, 0u};
    if (c16 == 12) vf[1] = v4u{0x3F803F80u, 0x3F803F80u, 0x3F803F80u, 0x3F803F80u};
#pragma unroll
    for (int dt = 0; dt < 2; ++dt)
#pragma unroll
      for (int et = 0; et < 2; ++et) kv[dt][et] = mfma(pk[dt], vf[et], kv[dt][et]);
  }
#pragma unroll
  for (int t = 0; t < 4; ++t) *(f32x4*)(lds + LB_R + (w * 4 + t) * 1024 + lane * 16) = kv[t >> 1][t & 1];
  lbar();
  if (w < 4) {
    f32x4 a = *(const f32x4*)(lds + LB_R + w * 1024 + lane * 16);
#pragma unroll
    for (int w2 = 1; w2 < 8; ++w2) a += *(const f32x4*)(lds + LB_R + (w2 * 4 + w) * 1024 + lane * 16);
    st128s(__builtin_bit_cast(v4u, a), c.rs, (int)(KVP_OFF + ((size_t)((h * 4 + tq) * 4 + w) * 64 + lane) * 16));
  }
  vdrain();
  lbar();
  if (tid == 0) cw_set(c, CW_B1 + h * 4 + tq, 1u);
}

constexpr int LB_OF = 0;
DI void task_B2(const Params& p, const Ctx& c, unsigned char* lds, int tt) {
  const int tid = threadIdx.x, lane = tid & 63, w = __builtin_amdgcn_readfirstlane(tid >> 6);
  const int gq = lane >> 4;
  const int c16 = lane & 15;
  if (w == 0) wait_vec(c, CW_B1 + lane, 1u, 4);
  lbar();
  v4u pq[2][4];
#pragma unroll
  for (int hh = 0; hh < 2; ++hh)
#pragma unroll
    for (int t = 0; t < 4; ++t) pq[hh][t] = ld128s(c.rs, (int)(PQ_OFF + ((size_t)((4 * tt + t) * 16 + 2 * w + hh) * 64 + lane) * 16));
  v4u kva[2][2];
#pragma unroll
  for (int hh = 0; hh < 2; ++hh)
#pragma unroll
    for (int et = 0; et < 2; ++et) {
      f32x4 d0 = {0, 0, 0, 0}, d1 = {0, 0, 0, 0};
#pragma unroll
      for (int tq = 0; tq < 4; ++tq) {
        d0 += __builtin_bit_cast(f32x4, ld128s(c.rs, (int)(KVP_OFF + ((size_t)(((2 * w + hh) * 4 + tq) * 4 + et) * 64 + lane) * 16)));
        d1 += __builtin_bit_cast(f32x4, ld128s(c.rs, (int)(KVP_OFF + ((size_t)(((2 * w + hh) * 4 + tq) * 4 + 2 + et) * 64 + lane) * 16)));
      }
      kva[hh][et] = v4u{pk2(d0[0], d0[1]), pk2(d0[2], d0[3]), pk2(d1[0], d1[1]), pk2(d1[2], d1[3])};
    }
#pragma unroll
  for (int hh = 0; hh < 2; ++hh)
#pragma unroll
    for (int t = 0; t < 4; ++t) {
      const f32x4 n0 = mfma(kva[hh][0], pq[hh][t], f32x4{0, 0, 0, 0}), n1 = mfma(kva[hh][1], pq[hh][t], f32x4{0, 0, 0, 0});
      float den = swap32_add(swap16_add(gq == 3 ? n1[0] : 0.f));
      den = bflo(pk2(bflo(pk2(den, 0.f)) + 1e-6f, 0.f));
      const float rd = 1.f / den;
      const unsigned a0 = pk2(n0[0], n0[1]), a1 = pk2(n0[2], n0[3]), b0 = pk2(n1[0], n1[1]), b1 = pk2(n1[2], n1[3]);
      v4u o = {pk2(bflo(a0) * rd, bfhi(a0) * rd), pk2(bflo(a1) * rd, bfhi(a1) * rd), pk2(bflo(b0) * rd, bfhi(b0) * rd), pk2(bflo(b1) * rd, bfhi(b1) * rd)};
      if (gq == 3) { o.z = 0u; o.w = 0u; }
      *(v4u*)(lds + LB_OF + ((2 * w + hh) * 4 + t) * 1024 + lane * 16) = o;
    }
  lbar();
  f32x4 acc[4][4];
#pragma unroll
  for (int j = 0; j < 4; ++j)
#pragma unroll
    for (int t = 0; t < 4; ++t) acc[j][t] = f32x4{0, 0, 0, 0};
  const int nj = w < 4 ? 4 : 3;
#pragma unroll 2
  for (int h = 0; h < 16; ++h) {
    v4u a[4], b[4];
#pragma unroll
    for (int j = 0; j < 4; ++j) a[j] = ld128s(c.rs, (int)(WOF_OFF + ((size_t)(h * 28 + (j < nj ? w + 8 * j : w)) * 64 + lane) * 16));
#pragma unroll
    for (int t = 0; t < 4; ++t) b[t] = *(const v4u*)(lds + LB_OF + (h * 4 + t) * 1024 + lane * 16);
#pragma unroll
    for (int j = 0; j < 4; ++j)
#pragma unroll
      for (int t = 0; t < 4; ++t) acc[j][t] = mfma(a[j], b[t], acc[j][t]);
  }
#pragma unroll
  for (int j = 0; j < 4; ++j)
    if (j < nj) {
#pragma unroll
      for (int t = 0; t < 4; ++t)
        *(v2u*)(p.out + (size_t)(64 * tt + 16 * t + c16) * (2 * D) + D + 16 * (w + 8 * j) + 4 * gq) = v2u{pk2(acc[j][t][0], acc[j][t][1]), pk2(acc[j][t][2], acc[j][t][3])};
    }
  lbar();
}

DI void role_W(const Params& p, const Ctx& c, unsigned char* lds, int wid) {
  if (threadIdx.x < 64) wait_vec(c, CW_PROG + wid * 16, 1u, 4);
  lbar();
  task_B1(p, c, lds, wid >> 2, wid & 3);
  if ((wid & 1) == 0) task_B2(p, c, lds, wid >> 1);
#pragma unroll 1
  for (int k = 0; k < 4; ++k) { task_T(p, c, lds, 16 * k + (wid >> 2), wid & 3); }
}

__global__ void __launch_bounds__(512, 2) k_main(Params p) {
  extern __shared__ __attribute__((aligned(16))) unsigned char lds[];
  const int b = blockIdx.x, label = b & 7, rank = b >> 3;
  const int tid = threadIdx.x, lane = tid & 63, w = __builtin_amdgcn_readfirstlane(tid >> 6);
  Ctx c;
  c.rs = __builtin_amdgcn_make_buffer_rsrc(p.ws, 0, (int)WS_USED, 0x00020000);
  c.cw = (GAS u64*)(p.ws + CTRL_OFF);
  c.cls = 0;
  const u64 old = __hip_atomic_load(c.cw + CW_EPOCH, RLX_AGENT);
  const bool isS = label < 2, isO = (label == 2 || label == 3) && rank < 14;
  const int sid = rank * 2 + label, oid = rank * 2 + (label - 2);
  const bool docal = label == 3 && rank == 31;
  unsigned best[4] = {1u << 30, 1u << 30, 1u << 30, 1u << 30};
  if (w == 7) {
    if (docal) { if (lane < 16) st32s((unsigned)lane, c.rs, (int)XCH_OFF + (24 + (lane >> 2)) * 32768 + (lane & 3) * 8192); }
    if (isS) {
      const int rec = sid * 512;
#pragma unroll
      for (int k = 0; k < 8; ++k)
        if (lane < 28) st128s(v4u{SENT, SENT, SENT, SENT}, c.rs, xch_q(k >> 2, 1 + (rec >> 13), k & 3) + (rec & 8191) + lane * 16);
    } else if (isO) {
      const int oc = oid, oks = oc >> 1, oksq = oks >= 7 ? oks - 7 : oks, ohf = oks >= 7 ? 1 : 0;
      const int pos = oksq * 1024 + ((2 * (oc & 1) + (lane >> 5)) * 16 + ohf * 8 + ((lane >> 2) & 7)) * 16 + (lane & 3) * 4;
#pragma unroll
      for (int k = 0; k < 8; ++k) st32s(SENT, c.rs, xch_q(1 + (k >> 2), 0, k & 3) + pos);
    }
  }
  {
    const int m = label * 4 + (rank >> 3), blk = rank & 7;
    if (m < 16) prep_block<true>(p, c, lds, m, blk >> 1, blk & 1, docal, best);
    else prep_block<false>(p, c, lds, m, blk >> 1, blk & 1, docal, best);
    const unsigned lo = __builtin_amdgcn_readfirstlane((unsigned)old), hi = __builtin_amdgcn_readfirstlane((unsigned)(old >> 32));
    c.tagw = ((((u64)hi << 32) | lo) + 1) << 8;
    vdrain();
    lbar();
    if (tid == 0) cw_set(c, CW_PF + (m * 8 + blk) * 16, 1u);
    if (docal && tid == 448) {
      const unsigned m01 = best[1] < best[0] ? best[1] : best[0], m23 = best[3] < best[2] ? best[3] : best[2];
      const unsigned q = m23 < m01 ? (best[3] < best[2] ? 3u : 2u) : (best[1] < best[0] ? 1u : 0u);
      cw_set(c, CW_CLASS, 1u | q << 1);
    }
  }
  if (isS) role_S(p, c, lds, sid >> 2, sid & 3);
  else if (isO) role_O(p, c, lds, oid);
  else if (label < 4) {}
  else if (rank < 16) role_KV(p, c, lds, rank * 4 + (label - 4));
  else role_W(p, c, lds, (rank - 16) * 4 + (label - 4));
}

static unsigned char* align_ws(void* d_ws) { return (unsigned char*)(((uintptr_t)d_ws + (2u << 20) - 1) & ~(uintptr_t)((2u << 20) - 1)); }
static void launch_v30(void* const* d_in, void* d_out, void* d_ws, hipStream_t stream) {
  Params p;
  p.x = (const u16*)d_in[0]; p.mem0 = (const u16*)d_in[1]; p.Wq = (const u16*)d_in[2]; p.Wk = (const u16*)d_in[3]; p.Wv = (const u16*)d_in[4]; p.Wo = (const u16*)d_in[5];
  p.bx = (const u16*)d_in[7]; p.wqkv = (const u16*)d_in[8]; p.wo = (const u16*)d_in[9];
  p.out = (u16*)d_out; p.ws = align_ws(d_ws);
  (void)hipFuncSetAttribute((const void*)k_main, hipFuncAttributeMaxDynamicSharedMemorySize, 128 * 1024);
  k_main<<<256, 512, 128 * 1024, stream>>>(p);
}
extern "C" void kernel_launch(void* const* d_in, const int* in_sizes, int n_in, void* d_out, int out_size, void* d_ws, size_t ws_size,
                              hipStream_t stream) {
  (void)in_sizes; (void)n_in; (void)out_size; (void)ws_size;
  launch_v30(d_in, d_out, d_ws, stream);
}
```

```cpp
#include <hip/hip_runtime.h>
#include <hip/hip_bf16.h>
#include <stdint.h>

typedef unsigned short u16;
typedef unsigned long long u64;
typedef unsigned v4u __attribute__((ext_vector_type(4)));
typedef unsigned v2u __attribute__((ext_vector_type(2)));
typedef unsigned v8u __attribute__((ext_vector_type(8)));
typedef float f32x4 __attribute__((ext_vector_type(4)));
typedef float f32x2 __attribute__((ext_vector_type(2)));
typedef short bf16x8 __attribute__((ext_vector_type(8)));
typedef __bf16 bf16x2_t __attribute__((ext_vector_type(2)));
typedef __bf16 bf16x8_t __attribute__((ext_vector_type(8)));
typedef __bf16 bf16x16_t __attribute__((ext_vector_type(16)));
typedef short v4s __attribute__((ext_vector_type(4)));
#define GAS __attribute__((address_space(1)))
#define LAS __attribute__((address_space(3)))
#define DI __device__ __forceinline__
#define RLX_AGENT __ATOMIC_RELAXED, __HIP_MEMORY_SCOPE_AGENT

constexpr int D = 448, H = 16, E = 256, NSEG = 64;
constexpr unsigned SENT = 0xFFFFFFFFu;
constexpr int AUX_SC1 = 16;
constexpr float LOG2E = 1.4426950408889634f;
constexpr unsigned SPIN_CAP = 1u << 20;
#define NAP_OM 6

constexpr size_t XCH_OFF = 0;
constexpr size_t CTRL_OFF = 1u << 20;
constexpr size_t WF_OFF = 2u << 20;
constexpr size_t KKF_OFF = 16u << 20;
constexpr size_t VVF_OFF = 46u << 20;
constexpr size_t KKM_OFF = 76u << 20;
constexpr size_t VMF_OFF = 84u << 20;
constexpr size_t SXX_OFF = 92u << 20;
constexpr size_t KVP_OFF = 97u << 20;
constexpr size_t SXM_OFF = (97u << 20) + (1u << 19);
constexpr size_t WOF_OFF = 98u << 20;
constexpr size_t PQ_OFF = 100u << 20;
constexpr size_t WS_USED = 128u << 20;
constexpr int CW_EPOCH = 0;
constexpr int CW_CLASS = 16;
constexpr int CW_TMO = 32;
constexpr int CW_RHO = 48;
constexpr int CW_PF = 64;
constexpr int CW_B1 = CW_PF + 256 * 16;
constexpr int CW_PROG = CW_B1 + 64;
constexpr int CW_PM = CW_PROG + 64 * 16;

DI unsigned pk2(float a, float b) { f32x2 v = {a, b}; bf16x2_t r = __builtin_convertvector(v, bf16x2_t); return __builtin_bit_cast(unsigned, r); }
DI f32x4 mfma(v4u a, v4u b, f32x4 c) { return __builtin_amdgcn_mfma_f32_16x16x32_bf16(__builtin_bit_cast(bf16x8, a), __builtin_bit_cast(bf16x8, b), c, 0, 0, 0); }
DI f32x4 smfmac(v4u a, v8u b, f32x4 c, int idx) { return __builtin_amdgcn_smfmac_f32_16x16x64_bf16(__builtin_bit_cast(bf16x8_t, a), __builtin_bit_cast(bf16x16_t, b), c, idx, 0, 0); }
DI f32x4 mfma16(v2u a, v2u b, f32x4 c) { return __builtin_amdgcn_mfma_f32_16x16x16bf16_1k(__builtin_bit_cast(v4s, a), __builtin_bit_cast(v4s, b), c, 0, 0, 0); }
DI v2u ldtr(const unsigned char* lds, int off) { return __builtin_bit_cast(v2u, __builtin_amdgcn_ds_read_tr16_b64_v4i16((LAS v4s*)((LAS unsigned char*)lds + off))); }
DI v4u ld128s(__amdgpu_buffer_rsrc_t rs, int off) { return __builtin_amdgcn_raw_buffer_load_b128(rs, off, 0, AUX_SC1); }
DI v2u ld64s(__amdgpu_buffer_rsrc_t rs, int off) { return __builtin_amdgcn_raw_buffer_load_b64(rs, off, 0, AUX_SC1); }
DI unsigned ld32s(__amdgpu_buffer_rsrc_t rs, int off) { return __builtin_amdgcn_raw_buffer_load_b32(rs, off, 0, AUX_SC1); }
DI void st128s(v4u v, __amdgpu_buffer_rsrc_t rs, int off) { __builtin_amdgcn_raw_buffer_store_b128(v, rs, off, 0, AUX_SC1); }
DI void st64s(v2u v, __amdgpu_buffer_rsrc_t rs, int off) { __builtin_amdgcn_raw_buffer_store_b64(v, rs, off, 0, AUX_SC1); }
DI void st32s(unsigned v, __amdgpu_buffer_rsrc_t rs, int off) { __builtin_amdgcn_raw_buffer_store_b32(v, rs, off, 0, AUX_SC1); }
DI unsigned canon(unsigned x) { return x < 0xFFFFFFFEu ? x : 0xFFFFFFFEu; }
DI unsigned umax(unsigned a, unsigned b) { return a > b ? a : b; }
DI unsigned mx4(v4u v) { return umax(umax(v.x, v.y), umax(v.z, v.w)); }
DI bool ok4(v4u v) { return mx4(v) != SENT; }
DI void lbar() { asm volatile("s_waitcnt lgkmcnt(0)\n\ts_barrier" ::: "memory"); }
DI void vdrain() { asm volatile("s_waitcnt vmcnt(0)" ::: "memory"); }
#define MEMBAR() asm volatile("" ::: "memory")
#define BLD(a, off, rs) asm volatile("buffer_load_dwordx4 %0, %1, %2, 0 offen sc1" : "+v"(a) : "v"(off), "s"(rs) : "memory")
#define BWAIT(n, a) asm volatile("s_waitcnt vmcnt(" #n ")" : "+v"(a) :: "memory")
DI float vmax(float a, float b) { return __builtin_elementwise_maximum(a, b); }
DI float vmax3(float a, float b, float c) { return vmax(vmax(a, b), c); }
DI float swap32_add(float x) { auto r = __builtin_amdgcn_permlane32_swap(__float_as_uint(x), __float_as_uint(x), false, false); return __uint_as_float(r[0]) + __uint_as_float(r[1]); }
DI float swap16_add(float x) { auto r = __builtin_amdgcn_permlane16_swap(__float_as_uint(x), __float_as_uint(x), false, false); return __uint_as_float(r[0]) + __uint_as_float(r[1]); }
DI float swap32_max(float x) { auto r = __builtin_amdgcn_permlane32_swap(__float_as_uint(x), __float_as_uint(x), false, false); return vmax(__uint_as_float(r[0]), __uint_as_float(r[1])); }
DI float swap16_max(float x) { auto r = __builtin_amdgcn_permlane16_swap(__float_as_uint(x), __float_as_uint(x), false, false); return vmax(__uint_as_float(r[0]), __uint_as_float(r[1])); }
DI float ror8(float x) { return __uint_as_float((unsigned)__builtin_amdgcn_update_dpp(0, (int)__float_as_uint(x), 0x128, 0xf, 0xf, false)); }
#define DPP3(op, d0, d1, s0, s1) asm volatile("s_nop 1\n\t" \
    op " %0, %2, %2 row_ror:4 row_mask:0xf bank_mask:0xf\n\t" op " %1, %3, %3 row_ror:4 row_mask:0xf bank_mask:0xf\n\ts_nop 0\n\t" \
    op " %0, %0, %0 row_ror:2 row_mask:0xf bank_mask:0xf\n\t" op " %1, %1, %1 row_ror:2 row_mask:0xf bank_mask:0xf\n\ts_nop 0\n\t" \
    op " %0, %0, %0 row_ror:1 row_mask:0xf bank_mask:0xf\n\t" op " %1, %1, %1 row_ror:1 row_mask:0xf bank_mask:0xf" \
    : "=&v"(d0), "=&v"(d1) : "v"(s0), "v"(s1))
#define DPP4(op, d0, d1, s0, s1) asm volatile("s_nop 1\n\t" \
    op " %0, %2, %2 row_ror:8 row_mask:0xf bank_mask:0xf\n\t" op " %1, %3, %3 row_ror:8 row_mask:0xf bank_mask:0xf\n\ts_nop 0\n\t" \
    op " %0, %0, %0 row_ror:4 row_mask:0xf bank_mask:0xf\n\t" op " %1, %1, %1 row_ror:4 row_mask:0xf bank_mask:0xf\n\ts_nop 0\n\t" \
    op " %0, %0, %0 row_ror:2 row_mask:0xf bank_mask:0xf\n\t" op " %1, %1, %1 row_ror:2 row_mask:0xf bank_mask:0xf\n\ts_nop 0\n\t" \
    op " %0, %0, %0 row_ror:1 row_mask:0xf bank_mask:0xf\n\t" op " %1, %1, %1 row_ror:1 row_mask:0xf bank_mask:0xf" \
    : "=&v"(d0), "=&v"(d1) : "v"(s0), "v"(s1))
template <int N> DI float ror(float x) { return __uint_as_float((unsigned)__builtin_amdgcn_update_dpp(0, (int)__float_as_uint(x), 0x120 + N, 0xf, 0xf, false)); }
DI float add1(float a, float b) { float r = a + b; asm volatile("" : "+v"(r)); return r; }
DI float ex2(float x) { return __builtin_amdgcn_exp2f(x); }
DI float bflo(unsigned u) { return __uint_as_float(u << 16); }
DI float bfhi(unsigned u) { return __uint_as_float(u & 0xffff0000u); }
DI int xch_q(int slot, int blk, int q) { return (slot * 5 + blk) * 32768 + q * 8192; }
#define RECX 0
DI int xch_off(int slot, int blk, int cls) { return xch_q(slot, blk, cls ^ RECX); }
struct Params {
  const u16* x; const u16* mem0; const u16* Wq; const u16* Wk; const u16* Wv; const u16* Wo;
  const u16* bx; const u16* wqkv; const u16* wo;
  u16* out; unsigned char* ws;
};
struct Ctx { __amdgpu_buffer_rsrc_t rs, ro; GAS u64* cw; u64 tagw; int cls, mx;
};
DI int mem_rd(const Ctx& c, int slot) { return xch_q(slot, 0, c.cls ^ c.mx); }
#define OUT_AUX 0

DI void cw_set(const Ctx& c, int idx, unsigned n) { __hip_atomic_store(c.cw + idx, c.tagw | n, RLX_AGENT); }
DI bool cw_ge(const Ctx& c, u64 v, unsigned want) { const u64 d = v - c.tagw; return d <= 255ull && (unsigned)d >= want; }
DI bool cw_tmo(const Ctx& c) { return __builtin_amdgcn_readfirstlane((int)cw_ge(c, __hip_atomic_load(c.cw + CW_TMO, RLX_AGENT), 1u)) != 0; }
DI bool wait_vec(const Ctx& c, int idx, unsigned want, int nap) {
  for (unsigned spins = 0;; ++spins) {
    const u64 v = __hip_atomic_load(c.cw + idx, RLX_AGENT);
    if (__all(cw_ge(c, v, want))) return true;
    if (spins > SPIN_CAP) { cw_set(c, CW_TMO, 1u); return false; }
    if ((spins & 255u) == 255u && cw_tmo(c)) return false;
    if (nap == 1) __builtin_amdgcn_s_sleep(1); else __builtin_amdgcn_s_sleep(4);
  }
}
DI unsigned min16(const Ctx& c, u64 word) {
  const u64 d = word - c.tagw;
  unsigned v = d <= 255ull ? (unsigned)d : 0u, o;
  o = (unsigned)__builtin_amdgcn_mov_dpp((int)v, 0x128, 0xf, 0xf, false); v = o < v ? o : v;
  o = (unsigned)__builtin_amdgcn_mov_dpp((int)v, 0x124, 0xf, 0xf, false); v = o < v ? o : v;
  o = (unsigned)__builtin_amdgcn_mov_dpp((int)v, 0x122, 0xf, 0xf, false); v = o < v ? o : v;
  o = (unsigned)__builtin_amdgcn_mov_dpp((int)v, 0x121, 0xf, 0xf, false); v = o < v ? o : v;
  { auto r = __builtin_amdgcn_permlane16_swap(v, v, false, false); v = r[0] < r[1] ? r[0] : r[1]; }
  return (unsigned)__builtin_amdgcn_readfirstlane((int)v);
}
DI unsigned peek_prog(const Ctx& c, int idx) { return min16(c, __hip_atomic_load(c.cw + idx, RLX_AGENT)); }
DI bool wait_prog(const Ctx& c, int idx, unsigned want, unsigned& seen) {
  if (seen >= want) return true;
  const bool ok = wait_vec(c, idx, want, 1);
  seen = ok ? peek_prog(c, idx) : want;
  return ok;
}
DI bool poll128(const Ctx& c, int off, v4u& m) {
  unsigned spins = 0;
  bool good = true;
  do {
    asm volatile("" ::: "memory");
    m = ld128s(c.rs, off);
    if (++spins > SPIN_CAP) { good = false; break; }
  } while (!ok4(m));
  if (!__all(good)) { cw_set(c, CW_TMO, 1u); return false; }
  return true;
}

#define TAIL_NAP 26
#define TAIL_NAP2 24
#define CAL_B0 1200ull
#define CAL_B1 1000ull
#define MX_ODD 2
#define MX_EVEN 0
#define FIRSTX 0
#define NAP_XK 11
#define NAP_XS 2
#define NAP_SS 2
#define STR2(x) #x
#define STR(x) STR2(x)
#define NAP_C 0
#define PEEK_AHEAD 5
#define NAP_E 0
#define NOP_E 8
#define NOP_C 14
#define NOP_OM 0
#define NOPS(n) ".rept " STR(n) "\n\ts_nop 3\n\t.endr\n\t"
DI void poll128x2(const Ctx& c, int off, v4u& m) {
  m = v4u{SENT, SENT, SENT, SENT};
  BLD(m, off, c.rs);
  __builtin_amdgcn_s_sleep(NAP_SS);
  int n = (int)SPIN_CAP;
  bool more;
#pragma nounroll
  do {
    more = --n > 0;
    __builtin_amdgcn_sched_barrier(0);
    BLD(m, off, c.rs);
    BWAIT(1, m);
  } while (!__all(ok4(m)) & more);
}

DI void poll128x2_lds(const Ctx& c, int off, v4u& m, unsigned char* dst) {
  m = v4u{SENT, SENT, SENT, SENT};
  BLD(m, off, c.rs);
  __builtin_amdgcn_s_sleep(NAP_SS);
  int n = (int)SPIN_CAP;
  bool more;
  unsigned mv;
#pragma nounroll
  do {
    more = --n > 0;
    __builtin_amdgcn_sched_barrier(0);
    BLD(m, off, c.rs);
    BWAIT(1, m);
    mv = mx4(m);
    asm volatile("" : "+v"(mv) :: "memory");
    *(v4u*)dst = m;
    __builtin_amdgcn_sched_barrier(0);
  } while (!__all(mv != SENT) & more);
}
DI void boot_stores(const Ctx& c, int lane, bool docal, int calo) {
  const int b = blockIdx.x;
  if (docal) { if (lane < 16) st32s((unsigned)lane, c.rs, (int)XCH_OFF + (24 + (lane >> 2)) * 32768 + (lane & 3) * 8192 + calo); }
#pragma unroll
  for (int k = 0; k < 2; ++k) {
    const int g = b * 80 + 64 * k + lane, ch = g >> 9;
    if (64 * k + lane < 80) st128s(v4u{SENT, SENT, SENT, SENT}, c.rs, (ch < 32 ? xch_q(ch >> 4, 1 + ((ch >> 2) & 3), ch & 3) : xch_q(1 + ((ch - 32) >> 2), 0, ch & 3)) + (g & 511) * 16);
  }
#pragma unroll
  for (int k = 0; k < 2; ++k) {
    const int g = b * 88 + 64 * k + lane;
    if (64 * k + lane < 88) st128s(v4u{SENT, SENT, SENT, SENT}, c.rs, g < 8192 ? (int)SXM_OFF + g * 16 : (int)VMF_OFF + (NSEG - 2) * 28 * 8 * 512 + (g - 8192) * 16);
  }
}
template <bool ISA>
DI void prep_block(const Params& p, const Ctx& c, unsigned char* lds, int m, int ra, int cb, bool docal, int calo, bool pub, u64 old, unsigned (&best)[4]) {
  const int tid = threadIdx.x, lane = tid & 63, w = __builtin_amdgcn_readfirstlane(tid >> 6);
  const int h = m & 15, r16 = lane & 15, gq = lane >> 4;
  constexpr int L_R = 0, L_L = 224 * 256, L_BOOT = 120 * 1024;
  const u16* rowsrc = ISA ? p.Wk : p.Wv;
  v4u gr[2][7], gl[2][4];
#pragma unroll
  for (int hf = 0; hf < 2; ++hf) {
#pragma unroll
    for (int i = 0; i < 7; ++i) { const int q = tid + 512 * i; gr[hf][i] = *(const v4u*)(rowsrc + ((size_t)(224 * cb + (q >> 4)) * H + h) * E + 128 * hf + (q & 15) * 8); }
#pragma unroll
    for (int i = 0; i < 4; ++i) {
      const int q = (i < 3 || tid < 256) ? tid + 512 * i : tid;
      if constexpr (ISA) gl[hf][i] = *(const v4u*)(p.Wq + ((size_t)(112 * ra + (q >> 4)) * H + h) * E + 128 * hf + (q & 15) * 8);
      else { const int e = q / 14, ch = q - e * 14; gl[hf][i] = *(const v4u*)(p.Wo + ((size_t)h * E + 128 * hf + e) * D + 112 * ra + ch * 8); }
    }
    __builtin_amdgcn_sched_barrier(0);
  }
  if (w == 7) boot_stores(c, lane, docal, calo);
  f32x4 acc[7][2];
#pragma unroll
  for (int t = 0; t < 7; ++t) { acc[t][0] = f32x4{0, 0, 0, 0}; acc[t][1] = f32x4{0, 0, 0, 0}; }
#pragma unroll
  for (int hf = 0; hf < 2; ++hf) {
    if (hf) lbar();
#pragma unroll
    for (int i = 0; i < 7; ++i) { const int q = tid + 512 * i; *(v4u*)(lds + L_R + (q >> 4) * 256 + ((q ^ (q >> 4)) & 15) * 16) = gr[hf][i]; }
#pragma unroll
    for (int i = 0; i < 4; ++i) {
      const int q = (i < 3 || tid < 256) ? tid + 512 * i : tid;
      if constexpr (ISA) *(v4u*)(lds + L_L + (q >> 4) * 256 + ((q ^ (q >> 4)) & 15) * 16) = gl[hf][i];
      else { const int e = q / 14, ch = q - e * 14; *(v4u*)(lds + L_L + e * 224 + (e >> 3) * 128 + ch * 16) = gl[hf][i]; }
    }
    lbar();
    if (w < 7) {
      v4u fa[2][2], fb[2][7];
      auto rda = [&](int es, int j) { return *(const v4u*)(lds + L_R + (32 * w + 16 * j + r16) * 256 + ((4 * es + gq) ^ r16) * 16); };
      auto rdb = [&](int es, int t) {
        if constexpr (ISA) return *(const v4u*)(lds + L_L + (16 * t + r16) * 256 + ((4 * es + gq) ^ r16) * 16);
        else {
          const int q = (lane & 15) >> 2, pp = lane & 3;
          const int base = L_L + (32 * es + 8 * gq + q) * 224 + (4 * es + gq) * 128 + (16 * t + 4 * pp) * 2;
          v4s lo = __builtin_amdgcn_ds_read_tr16_b64_v4i16((LAS v4s*)((LAS unsigned char*)lds + base));
          v4s hi = __builtin_amdgcn_ds_read_tr16_b64_v4i16((LAS v4s*)((LAS unsigned char*)lds + base + 4 * 224));
          v2u l2 = __builtin_bit_cast(v2u, lo), h2 = __builtin_bit_cast(v2u, hi);
          return v4u{l2.x, l2.y, h2.x, h2.y};
        }
      };
      auto put = [&](int t, float sc, int gq2) {
        unsigned x0 = pk2(acc[t][0][0] * sc, acc[t][0][1] * sc), x1 = pk2(acc[t][0][2] * sc, acc[t][0][3] * sc);
        unsigned y0 = pk2(acc[t][1][0] * sc, acc[t][1][1] * sc), y1 = pk2(acc[t][1][2] * sc, acc[t][1][3] * sc);
        auto s0 = __builtin_amdgcn_permlane16_swap(x0, y0, false, false);
        auto s1 = __builtin_amdgcn_permlane16_swap(x1, y1, false, false);
        st128s(v4u{s0[0], s1[0], s0[1], s1[1]}, c.rs, (int)(WF_OFF + ((size_t)((m * 28 + 7 * ra + t) * 14 + 7 * cb + w) * 64 + r16 + 16 * gq2) * 16));
      };
      if (hf == 0) {
        fa[0][0] = rda(0, 0); fa[0][1] = rda(0, 1);
#pragma unroll
        for (int t = 0; t < 7; ++t) fb[0][t] = rdb(0, t);
#pragma unroll
        for (int es = 0; es < 4; ++es) {
#pragma unroll
          for (int t = 0; t < 7; ++t) {
            __builtin_amdgcn_sched_barrier(0);
            if (es < 3) {
              if (t < 2) fa[(es + 1) & 1][t] = rda(es + 1, t);
              fb[(es + 1) & 1][t] = rdb(es + 1, t);
            }
            __builtin_amdgcn_sched_barrier(0);
            acc[t][0] = mfma(fa[es & 1][0], fb[es & 1][t], acc[t][0]); acc[t][1] = mfma(fa[es & 1][1], fb[es & 1][t], acc[t][1]);
          }
        }
        __builtin_amdgcn_sched_barrier(0);
      } else {
        v4u ga[4][2], gb[2][4];
#pragma unroll
        for (int es = 0; es < 4; ++es) { ga[es][0] = rda(es, 0); ga[es][1] = rda(es, 1); gb[0][es] = rdb(es, 0); }
        const float sc = ISA ? 0.0625f * LOG2E : 1.0f;
        const int gq2 = (gq >> 1) + 2 * (gq & 1);
#pragma unroll
        for (int t = 0; t < 7; ++t) {
#pragma unroll
          for (int es = 0; es < 4; ++es) {
            __builtin_amdgcn_sched_barrier(0);
            if (t < 6) gb[(t + 1) & 1][es] = rdb(es, t + 1);
            __builtin_amdgcn_sched_barrier(0);
            acc[t][0] = mfma(ga[es][0], gb[t & 1][es], acc[t][0]); acc[t][1] = mfma(ga[es][1], gb[t & 1][es], acc[t][1]);
            if (es == 1 && t > 0) put(t - 1, sc, gq2);
          }
        }
        __builtin_amdgcn_sched_barrier(0);
        put(6, sc, gq2);
      }
    } else {
      asm volatile("" : "+v"(old));
      const u64 tagw = (old + 1) << 8;
      if (pub && hf == 0) {
        unsigned xcc; asm volatile("s_getreg_b32 %0, hwreg(HW_REG_XCC_ID)" : "=s"(xcc));
        if (lane == 0) __hip_atomic_store(c.cw + CW_RHO, tagw | 1u | (xcc & 7u) << 1, RLX_AGENT);
      }
      if (hf == 1) {
        u64 d = 0;
        for (unsigned spins = 0; spins < SPIN_CAP; ++spins) {
          d = __hip_atomic_load(c.cw + CW_RHO, RLX_AGENT) - tagw;
          if (d >= 1 && d <= 255) break;
          __builtin_amdgcn_s_sleep(2);
        }
        if (lane == 0) { *(u64*)(lds + L_BOOT) = tagw; *(unsigned*)(lds + L_BOOT + 8) = (unsigned)(d >> 1) & 7u; }
        docal = docal && ((__builtin_amdgcn_readfirstlane((unsigned)(d >> 1)) + blockIdx.x) & 7) == 3;
      }
      if (docal) {
      const unsigned long long tb = __builtin_amdgcn_s_memtime();
#pragma unroll 1
      for (int rep = 0; rep < 8; ++rep) {
        const int q = rep & 3;
        if ((hf || rep >= 4) && __builtin_amdgcn_s_memtime() - tb > (hf ? CAL_B1 : CAL_B0)) break;
        vdrain();
        const unsigned long long t0 = __builtin_amdgcn_s_memtime();
        const unsigned x = ld32s(c.rs, (int)XCH_OFF + (24 + 2 * hf + (rep >> 2)) * 32768 + q * 8192 + calo);
        asm volatile("s_waitcnt vmcnt(0)" :: "v"(x) : "memory");
        const unsigned d = (unsigned)(__builtin_amdgcn_s_memtime() - t0);
        if (q == 0) best[0] = d < best[0] ? d : best[0]; else if (q == 1) best[1] = d < best[1] ? d : best[1]; else if (q == 2) best[2] = d < best[2] ? d : best[2]; else best[3] = d < best[3] ? d : best[3];
      }
      }
    }
  }
}
DI v4u ld_wf(const Ctx& c, int tile, int ks, int lane) { return ld128s(c.rs, (int)(WF_OFF + ((size_t)(tile * 14 + ks) * 64 + lane) * 16)); }
DI v8u cat8(v4u a, v4u b) { return v8u{a.x, a.y, a.z, a.w, b.x, b.y, b.z, b.w}; }
DI v8u ld_wf2(const Ctx& c, int tile, int k, int lane) { return cat8(ld_wf(c, tile, 2 * k, lane), ld_wf(c, tile, 2 * k + 1, lane)); }
DI v4u ld_mem0(const Params& p, int k, int lane) {
  const unsigned* m = (const unsigned*)(p.mem0 + ((lane >> 1) & 7) * D + 64 * k + 16 * (lane >> 4) + 2 * (lane & 1));
  return v4u{m[0], m[2], m[4], m[6]};
}
constexpr int L_CLS = 15 * 1024, L_SEEN = L_CLS + 64;
DI bool wait_ring(Ctx& c, unsigned char* lds, int lane) {
  const bool ok = wait_vec(c, CW_CLASS, 1u, 1);
  const unsigned cls = (unsigned)(__hip_atomic_load(c.cw + CW_CLASS, RLX_AGENT) >> 1) & 3u;
  if (lane == 0) { *(unsigned*)(lds + L_CLS) = cls; *(unsigned*)(lds + L_SEEN) = 0u; }
  return ok;
}
DI void get_class(Ctx& c, const unsigned char* lds) { c.cls = __builtin_amdgcn_readfirstlane(*(const int*)(lds + L_CLS)); }

constexpr int L_MEM = 0;
constexpr int L_C = 8192;
constexpr int L_U = 8192;

DI void role_S(const Params& p, Ctx& c, unsigned char* lds, int h, int g) {
  const int tid = threadIdx.x, lane = tid & 63, w = __builtin_amdgcn_readfirstlane(tid >> 6);
  const int gq = lane >> 4, c16 = lane & 15;
  bool alive = true;
  if (w < 4) {
    alive = wait_vec(c, CW_PF + (h * 8 + 2 * g + (lane & 1)) * 16, 1u, 1);
    v8u wa[2][7];
#pragma unroll
    for (int t2 = 0; t2 < 2; ++t2)
#pragma unroll
      for (int k = 0; k < 7; ++k) {
        const int tile = (w == 3 && t2 == 1) ? 6 : 2 * w + t2;
        wa[t2][k] = ld_wf2(c, h * 28 + 7 * g + tile, k, lane);
      }
    __builtin_amdgcn_s_setprio(2);
    const int idx = (lane & 1) ? 0xEEEE : 0x4444;
    const int uaddr = L_U + (32 * w + c16) * 16 + gq * 4;
    *(v4u*)(lds + L_MEM + w * 1024 + lane * 16) = ld_mem0(p, w, lane);
    for (int s = 0; s < NSEG; ++s) {
      v4u m = {0u, 0u, 0u, 0u};
      f32x4 a = {0, 0, 0, 0}, b = {0, 0, 0, 0};
      asm volatile("" : "+v"(a), "+v"(b));
      if (s > 0) {
        poll128x2_lds(c, mem_rd(c, s & 3) + w * 1024 + lane * 16, m, lds + L_MEM + w * 1024 + lane * 16);
      }
      lbar();
      v4u mf[7];
#pragma unroll
      for (int k = 0; k < 7; ++k) mf[k] = *(const v4u*)(lds + L_MEM + k * 1024 + lane * 16);
      if (w < 3) {
#pragma unroll
        for (int k = 0; k < 7; ++k) { a = smfmac(mf[k], wa[0][k], a, idx); b = smfmac(mf[k], wa[1][k], b, idx); }
        *(unsigned*)(lds + uaddr) = pk2(add1(a[0], a[1]), add1(a[2], a[3]));
        *(unsigned*)(lds + uaddr + 256) = pk2(add1(b[0], b[1]), add1(b[2], b[3]));
      } else {
#pragma unroll
        for (int k = 0; k < 7; ++k) a = smfmac(mf[k], wa[0][k], a, idx);
        *(unsigned*)(lds + uaddr) = pk2(add1(a[0], a[1]), add1(a[2], a[3]));
      }
      lbar();
      if (s == 0) get_class(c, lds);
      BWAIT(0, m);
      if (!alive || !__all(ok4(m))) { cw_set(c, CW_TMO, 1u); break; }
    }
    lbar();
  } else if (w == 7) {
    const int rec = (h * 4 + g) * 512, recblk = 1 + (rec >> 13), recin = rec & 8191;
    int maddr[4];
#pragma unroll
    for (int u = 0; u < 4; ++u) {
      const int a = 112 * g + 32 * u + (u < 3 ? 8 : 4) * gq;
      maddr[u] = L_MEM + (a >> 6) * 1024 + (((a >> 4) & 3) * 16 + 2 * (c16 & 7)) * 16 + (a & 15);
    }
    const int utr = L_U + (8 * gq + ((c16 >> 2) & 1) + 4 * (c16 >> 3)) * 16 + (c16 & 1) * 8, utr3 = L_U + (96 + 4 * gq + (c16 >> 2)) * 16 + (c16 & 1) * 8;
    __builtin_amdgcn_s_setprio(3);
    v4u xa[4][3]; v2u xb[4];
#pragma unroll
    for (int tt = 0; tt < 4; ++tt) {
      const u16* xr = p.x + (size_t)(32 * (NSEG - 2) + 16 * tt + c16) * D + 112 * g;
#pragma unroll
      for (int u = 0; u < 3; ++u) xa[tt][u] = *(const v4u*)(xr + 32 * u + 8 * gq);
      xb[tt] = *(const v2u*)(xr + 96 + 4 * gq);
    }
    auto rd_kk = [&](v4u (&au)[3], v2u& a3) {
#pragma unroll
      for (int u = 0; u < 3; ++u) { const v2u lo = ldtr(lds, utr + u * 512), hi = ldtr(lds, utr + u * 512 + 32); au[u] = v4u{lo.x, lo.y, hi.x, hi.y}; }
      a3 = ldtr(lds, utr3);
    };
    auto put_scores = [&](const v4u (&au)[3], v2u a3, int k) {
      auto ord = [](v4u n) { return v4u{n.x, n.z, n.y, n.w}; };
#pragma unroll
      for (int tt = 0; tt < 2; ++tt) {
        f32x4 o = mfma(au[0], ord(xa[2 * k + tt][0]), f32x4{0, 0, 0, 0}), o2 = mfma(au[1], ord(xa[2 * k + tt][1]), f32x4{0, 0, 0, 0});
        o = mfma(au[2], ord(xa[2 * k + tt][2]), o); o2 = mfma16(a3, xb[2 * k + tt], o2);
        o += o2;
        const v4u ou = __builtin_bit_cast(v4u, o);
        if (gq < 2) st128s(v4u{canon(ou.x), canon(ou.y), canon(ou.z), canon(ou.w)}, c.rs, (int)SXM_OFF + k * 65536 + (h * 4 + g) * 1024 + ((16 * tt + c16) * 8 + 4 * gq) * 4);
      }
    };
    for (int s = 0; s < NSEG - 1; ++s) {
      const int slot = s & 3;
      lbar();
      if (s == 0) get_class(c, lds);
      v4u mb[3];
#pragma unroll
      for (int u = 0; u < 3; ++u) { const v2u r0 = *(const v2u*)(lds + maddr[u]), r1 = *(const v2u*)(lds + maddr[u] + 16); mb[u] = v4u{r0.x, r0.y, r1.x, r1.y}; }
      const v2u mb3 = v2u{*(const unsigned*)(lds + maddr[3]), *(const unsigned*)(lds + maddr[3] + 16)};
      int ro = xch_off(slot, recblk, c.cls) + recin + c16 * 16 + gq * 8;
      asm volatile("" : "+v"(ro), "+v"(mb[0]), "+v"(mb[1]), "+v"(mb[2]));
      lbar();
      v4u au[3]; v2u a3;
      rd_kk(au, a3);
      __builtin_amdgcn_sched_barrier(0);
      f32x4 o = mfma(au[0], mb[0], f32x4{0, 0, 0, 0}), o2 = mfma(au[1], mb[1], f32x4{0, 0, 0, 0});
      o = mfma(au[2], mb[2], o); o2 = mfma16(a3, mb3, o2);
      o += o2;
      if (__builtin_expect(c16 < 8 && gq < 2, 1)) st64s(v2u{canon(pk2(o[0], o[1])), canon(pk2(o[2], o[3]))}, c.rs, ro);
      if (lane < 8) st128s(v4u{SENT, SENT, SENT, SENT}, c.rs, xch_off((s + 2) & 3, recblk, c.cls) + recin + lane * 16);
      if (s == NSEG - 2) put_scores(au, a3, 0);
      else if (c16 < 8) {
        const int cb = (s * 16 + h) * 56 + 14 * g;
#pragma unroll
        for (int u = 0; u < 3; ++u) st128s(v4u{au[u].x, au[u].z, au[u].y, au[u].w}, c.rs, (int)(KKM_OFF + (size_t)(cb + 4 * u + gq) * 128 + c16 * 16));
        st64s(a3, c.rs, (int)(KKM_OFF + (size_t)(cb + 12 + (gq >> 1)) * 128 + c16 * 16 + (gq & 1) * 8));
      }
      vdrain();
      if (lane == 0 && alive) cw_set(c, CW_PM + (h * 4 + g) * 16, (unsigned)s + 1);
    }
    {
      lbar();
      lbar();
      v4u au[3]; v2u a3;
      rd_kk(au, a3);
      put_scores(au, a3, 1);
    }
    lbar();
    if (h == 0 && g == 0 && lane == 0) __hip_atomic_store(c.cw + CW_EPOCH, c.tagw >> 8, RLX_AGENT);
  } else {
    *(v4u*)(lds + L_MEM + w * 1024 + lane * 16) = ld_mem0(p, w, lane);
    if (w == 6) alive = wait_ring(c, lds, lane);
    for (int s = 0; s < NSEG; ++s) {
      v4u m = {0u, 0u, 0u, 0u};
      if (s > 0) {
        poll128x2_lds(c, mem_rd(c, s & 3) + w * 1024 + lane * 16, m, lds + L_MEM + w * 1024 + lane * 16);
      }
      lbar();
      BWAIT(0, m);
      if (!__all(ok4(m))) alive = false;
      if (s == 0) get_class(c, lds);
      lbar();
      if (!alive) { cw_set(c, CW_TMO, 1u); break; }
    }
    lbar();
  }
}

DI void role_X(const Params& p, Ctx& c, unsigned char* lds, int h) {
  const int tid = threadIdx.x, lane = tid & 63, w = __builtin_amdgcn_readfirstlane(tid >> 6);
  const int gq = lane >> 4, c16 = lane & 15;
  if (w > 3) return;
  constexpr int L_XP = 0;
  const int idx = (lane & 1) ? 0xEEEE : 0x4444;
  const int k1 = w < 3 ? 2 * w + 1 : 6;
  bool alive = true;
  unsigned kkprog = 0;
  v8u kf[2][2];
  auto load_kk = [&](int s) -> bool {
    if (s > 0 && kkprog < (unsigned)s + 1) { const unsigned sn = (unsigned)__builtin_amdgcn_readfirstlane((int)*(volatile LAS unsigned*)((LAS unsigned char*)lds + L_SEEN)); kkprog = sn > kkprog ? sn : kkprog; }
    const bool ok = wait_prog(c, CW_PROG + (2 * h + (lane & 1)) * 16 + (lane & 2) * 4, s + 1, kkprog);
#pragma unroll
    for (int t = 0; t < 2; ++t)
#pragma unroll
      for (int k = 0; k < 2; ++k) {
        const int o = (int)(KKF_OFF + ((size_t)(((s * 16 + h) * 4 + (c16 >> 2)) * 14 + 2 * (k ? k1 : 2 * w)) * 32 + gq * 8 + 2 * (c16 & 3) + t) * 16);
        kf[t][k] = cat8(ld128s(c.rs, o), ld128s(c.rs, o + 512));
      }
#pragma unroll
    for (int k = 0; k < 2; ++k) asm volatile("" :: "v"(kf[0][k]), "v"(kf[1][k]));
    return ok;
  };
  v4u ma, mb;
  { const v4u t = ld_mem0(p, 2 * w, lane), u = ld_mem0(p, k1, lane);
    ma = v4u{canon(t.x), canon(t.y), canon(t.z), canon(t.w)}; mb = v4u{canon(u.x), canon(u.y), canon(u.z), canon(u.w)}; }
  if (w == 3) alive = wait_ring(c, lds, lane);
  lbar();
  get_class(c, lds);
  alive = load_kk(0) && alive;
  const int rec0 = h * 2048, recblk = 1 + (rec0 >> 13), recin = rec0 & 8191;
  unsigned pprog = 0;
  const int pko = (int)CTRL_OFF + (CW_PROG + (2 * h + (lane & 1)) * 16 + (lane & 2) * 4) * 8;
#define BLD2(q) asm volatile("s_cmp_eq_u32 %5, 0\n\ts_cbranch_scc1 1f\n\tbuffer_load_dwordx4 %0, %2, %4, 0 offen sc1\n\tbuffer_load_dwordx4 %1, %3, %4, 0 offen sc1\n1:" \
        : "+v"(ma), "+v"(mb) : "v"(mo), "v"(mo2), "s"(c.rs), "s"(q) : "memory", "scc")
#define BWAIT2(n) asm volatile("s_waitcnt vmcnt(" #n ")" : "+v"(ma), "+v"(mb) :: "memory")
  for (int s = 0; s < NSEG - 1; ++s) {
    const int slot = s & 3;
    const int mo = mem_rd(c, slot) + 2 * w * 1024 + lane * 16, mo2 = mem_rd(c, slot) + k1 * 1024 + lane * 16;
    int eo = xch_off(slot, recblk, c.cls) + recin + (c16 >> 2) * 512 + 256 + 2 * gq * 16 + (c16 & 3) * 4;
    asm volatile("" : "+v"(eo));
    int look = __builtin_amdgcn_readfirstlane((int)(w == 3 && pprog < (unsigned)s + 6 && pprog < (unsigned)NSEG));
    asm volatile("" : "+s"(look));
    v2u pk = {0u, 0u};
    asm volatile("s_cmp_eq_u32 %2, 0\n\ts_cbranch_scc1 1f\n\tbuffer_load_dwordx2 %0, %1, %3, 0 offen sc1\n1:" : "+v"(pk) : "v"(pko), "s"(look), "s"(c.rs) : "memory", "scc");
    BLD2(s);
    __builtin_amdgcn_s_sleep(NAP_XS);
    {
      int n = (int)SPIN_CAP;
      bool more;
#pragma nounroll
      do {
        more = --n > 0;
        __builtin_amdgcn_sched_barrier(0);
        BLD2(s);
        BWAIT2(2);
      } while (!__all(umax(mx4(ma), mx4(mb)) != SENT) & more);
    }
    f32x4 a0 = smfmac(ma, kf[0][0], f32x4{0, 0, 0, 0}, idx), a1 = smfmac(ma, kf[1][0], f32x4{0, 0, 0, 0}, idx);
    if (w < 3) { a0 = smfmac(mb, kf[0][1], a0, idx); a1 = smfmac(mb, kf[1][1], a1, idx); }
    f32x4 pt = {add1(a0[0], a0[1]), add1(a0[2], a0[3]), add1(a1[0], a1[1]), add1(a1[2], a1[3])};
    unsigned char* xp = lds + L_XP + (s & 1) * 4096 + lane * 16;
    if (w > 0) *(f32x4*)(xp + w * 1024) = pt;
    lbar();
    if (w == 0) {
      pt += *(const f32x4*)(xp + 1024) + (*(const f32x4*)(xp + 2048) + *(const f32x4*)(xp + 3072));
      const float x0 = pt[0], y0 = pt[1], x1 = pt[2], y1 = pt[3];
      float mx, my;
      DPP4("v_max_f32_dpp", mx, my, vmax(x0, x1), vmax(y0, y1));
      st32s(canon(pk2(ex2(x0 - mx), ex2(x1 - mx))), c.rs, eo);
      st32s(canon(pk2(ex2(y0 - my), ex2(y1 - my))), c.rs, eo + 16);
      if (c16 == 0) st64s(v2u{canon(__float_as_uint(mx)), canon(__float_as_uint(my))}, c.rs, eo + 128 - gq * 24);
      const int rr = xch_off((s + 2) & 3, recblk, c.cls) + recin;
      if (lane < 34) st128s(v4u{SENT, SENT, SENT, SENT}, c.rs, rr + (lane < 32 ? (lane >> 3) * 512 + 256 + (lane & 7) * 16 : 384 + (lane - 32) * 16));
    }
    asm volatile("s_cmp_eq_u32 %2, 0\n\ts_cbranch_scc1 1f\n\ts_waitcnt vmcnt(0)\n\ts_branch 2f\n1:\n\ts_waitcnt vmcnt(4)\n2:" : "+v"(ma), "+v"(mb) : "s"(w) : "memory", "scc");
    if (!__all(umax(mx4(ma), mx4(mb)) != SENT)) alive = false;
    ma = v4u{SENT, SENT, SENT, SENT}; mb = v4u{SENT, SENT, SENT, SENT};
    if (look) {
      asm volatile("" : "+v"(pk));
      pprog = min16(c, ((u64)pk.y << 32) | pk.x);
      if (lane == 0) *(volatile LAS unsigned*)((LAS unsigned char*)lds + L_SEEN) = pprog;
    }
    if (w > 0) __builtin_amdgcn_s_sleep(NAP_XK);
    if (s + 1 < NSEG - 1) alive = load_kk(s + 1) && alive;
    if (!alive) { cw_set(c, CW_TMO, 1u); break; }
  }
}

DI void role_O(const Params& p, Ctx& c, unsigned char* lds, int oc) {
  const int tid = threadIdx.x, lane = tid & 63, w = __builtin_amdgcn_readfirstlane(tid >> 6);
  const int gq = lane >> 4, c16 = lane & 15;
  bool alive = wait_vec(c, CW_PF + ((16 + 2 * w + (lane & 1)) * 8 + 2 * (oc / 7) + ((lane >> 1) & 1)) * 16, 1u, 1);
  v8u wb[2][7];
#pragma unroll
  for (int hh = 0; hh < 2; ++hh)
#pragma unroll
    for (int k = 0; k < 7; ++k) wb[hh][k] = ld_wf2(c, 448 + (2 * w + hh) * 28 + oc, k, lane);
  const int idx = (lane & 1) ? 0xEEEE : 0x4444;
  if (w < 7) *(v4u*)(lds + L_MEM + w * 1024 + lane * 16) = ld_mem0(p, w, lane);
  const int half = oc >= 14 ? 1 : 0;
  unsigned vprog = 0;
  const int vpw = CW_PROG + (32 + 2 * (2 * w + ((lane >> 1) & 1)) + half) * 16 + (lane & 1) * 8;
  const int vo = (int)(VVF_OFF + ((size_t)(oc * 16 + 2 * w) * 64 + lane) * 16);
  v4u vx[2];
#define BLDV(a, s_, q) asm volatile("s_cmp_eq_u32 %4, 0\n\ts_cbranch_scc1 1f\n\tbuffer_load_dwordx4 %0, %2, %3, 0 offen sc1\n\tbuffer_load_dwordx4 %1, %2, %3, 0 offen offset:1024 sc1\n1:" \
    : "+v"(a[0]), "+v"(a[1]) : "v"(vo + (s_) * (28 * 16 * 1024)), "s"(c.rs), "s"(q) : "memory", "scc")
  alive = wait_prog(c, vpw, 1, vprog) && alive;
#pragma unroll
  for (int hh = 0; hh < 2; ++hh) vx[hh] = ld128s(c.rs, vo + hh * 1024);
  if (w == 6) alive = wait_ring(c, lds, lane) && alive;
  const int rh = 2 * w + ((lane >> 3) & 1), ri = lane & 7;
  const int rec = (rh * 4 + gq) * 512, recblk = 1 + (rec >> 13), recin = rec & 8191;
  v4u sel[2];
#pragma unroll
  for (int t = 0; t < 2; ++t) {
    const int q = c16 >> 2, e = c16 & 3, j = 2 * (q ^ (2 * t + (e >> 1))) + (e & 1);
#pragma unroll
    for (int k = 0; k < 4; ++k) sel[t][k] = j == 2 * k ? 0x00003F80u : (j == 2 * k + 1 ? 0x3F800000u : 0u);
  }
  if (w >= 4) __builtin_amdgcn_s_setprio(1);
  int pend = 0;
  unsigned vseen = 0, pprog = 0;
  const int pko = (int)CTRL_OFF + (CW_PROG + (32 + 2 * (lane & 15) + half) * 16 + ((lane >> 4) & 1) * 8) * 8;
  v2u zb = {0u, 0u};
  for (int s = 0; s < NSEG; ++s) {
    const int slot = s & 3;
    if (pend) alive = wait_prog(c, vpw, s + 1, vprog) && alive;
    BLDV(vx, s, pend);
    if (pend && w == 7) vdrain();
    v4u m = {0u, 0u, 0u, 0u};
    int look = __builtin_amdgcn_readfirstlane((int)(w == 5 && s > 0 && pprog < (unsigned)s + PEEK_AHEAD && pprog < (unsigned)NSEG));
    asm volatile("" : "+s"(look));
    v2u pk = {0u, 0u};
    if (w < 7 && s > 0) {
      asm volatile("s_cmp_eq_u32 %2, 0\n\ts_cbranch_scc1 1f\n\tbuffer_load_dwordx2 %0, %1, %3, 0 offen sc1\n1:" : "+v"(pk) : "v"(pko), "s"(look), "s"(c.rs) : "memory", "scc");
      __builtin_amdgcn_s_sleep(NAP_OM);
      if (NOP_OM > 0) asm volatile(NOPS(NOP_OM));
      poll128x2(c, mem_rd(c, slot) + w * 1024 + lane * 16, m);
      *(v4u*)(lds + L_MEM + w * 1024 + lane * 16) = m;
    }
    lbar();
    if (s == 0) get_class(c, lds);
    if (s > 0) st64s(zb, c.rs, (int)(VMF_OFF + ((size_t)(((s - 1) * 28 + oc) * 8 + w) * 64 + lane) * 8));
    int go = 0;
    if (s + 1 < NSEG - 1) {
      if (vprog < (unsigned)s + 2) { vseen = (unsigned)__builtin_amdgcn_readfirstlane(*(const int*)(lds + L_SEEN)); vprog = vseen > vprog ? vseen : vprog; }
      go = __builtin_amdgcn_readfirstlane((int)(vprog >= (unsigned)s + 2));
      pend = go ^ 1;
    } else pend = 0;
    asm volatile("" : "+s"(go), "+s"(pend));
    v4u mf[7];
#pragma unroll
    for (int k = 0; k < 7; ++k) mf[k] = *(const v4u*)(lds + L_MEM + k * 1024 + lane * 16);
    __builtin_amdgcn_sched_barrier(0);
    {
      f32x4 z0 = {0, 0, 0, 0}, z1 = {0, 0, 0, 0};
#pragma unroll
      for (int k = 0; k < 7; ++k) { z0 = smfmac(mf[k], wb[0][k], z0, idx); z1 = smfmac(mf[k], wb[1][k], z1, idx); }
      zb = v2u{canon(pk2(add1(z0[0], z0[1]), add1(z0[2], z0[3]))), canon(pk2(add1(z1[0], z1[1]), add1(z1[2], z1[3])))};
    }
    asm volatile("" :: "v"(zb));
    BWAIT(1, m);
    if (!__all(ok4(m))) alive = false;
    if (s < NSEG - 1) {
      const int ro = xch_off(slot, recblk, c.cls) + recin;
      const int so = ro + ri * 16, mo = ro - gq * 512 + 384 + ri * 4;
      v4u ca = {SENT, SENT, SENT, SENT}, cb = {SENT, SENT, SENT, SENT}, c2 = {SENT, SENT, SENT, SENT}; unsigned c3 = SENT;
      if (look) {
        asm volatile("" : "+v"(pk));
        pprog = min16(c, ((u64)pk.y << 32) | pk.x);
        if (lane == 0) *(unsigned*)(lds + L_SEEN) = pprog;
      } else { if (NAP_E > 0) __builtin_amdgcn_s_sleep(NAP_E); if (NOP_E > 0) asm volatile(NOPS(NOP_E)); }
      asm volatile("buffer_load_dword %2, %4, %5, 0 offen sc1\n\tbuffer_load_dwordx4 %1, %3, %5, 0 offen offset:256 sc1\n\t"
                   "s_sleep " STR(NAP_C) "\n\t" NOPS(NOP_C) "buffer_load_dwordx4 %0, %3, %5, 0 offen sc1\n\ts_waitcnt vmcnt(1)"
                   : "+v"(ca), "+v"(c2), "+v"(c3) : "v"(so), "v"(mo), "s"(c.rs) : "memory");
#pragma nounroll
      for (unsigned spins = 0; !__all(umax(mx4(c2), c3) != SENT); ++spins) {
        asm volatile("buffer_load_dword %1, %3, %4, 0 offen sc1\n\tbuffer_load_dwordx4 %0, %2, %4, 0 offen offset:256 sc1\n\ts_waitcnt vmcnt(0)"
                     : "+v"(c2), "+v"(c3) : "v"(so), "v"(mo), "s"(c.rs) : "memory");
        if (spins > SPIN_CAP) { cw_set(c, CW_TMO, 1u); alive = false; break; }
      }
      BLD(cb, so, c.rs);
      __builtin_amdgcn_sched_barrier(0);
      const float mx = __uint_as_float(c3);
      const f32x4 oa = mfma(vx[0], c2, f32x4{0, 0, 0, 0}), ob = mfma(vx[1], c2, f32x4{0, 0, 0, 0});
      const float lx = mfma(v4u{0x3F803F80u, 0x3F803F80u, 0x3F803F80u, 0x3F803F80u}, c2, f32x4{0, 0, 0, 0})[0];
      f32x4 ox;
#pragma unroll
      for (int r = 0; r < 4; ++r) ox[r] = c16 < 8 ? oa[r] : ob[r];
      asm volatile("" :: "v"(ox), "v"(lx), "v"(mx));
      BWAIT(1, ca);
      f32x4 sc = mfma(sel[0], ca, f32x4{0, 0, 0, 0}), sd = mfma(sel[1], ca, f32x4{0, 0, 0, 0});
      if (__builtin_expect(!__all(ok4(ca)), 0)) {
        BWAIT(0, cb);
        v4u c1 = cb;
#pragma nounroll
        for (unsigned spins = 0; !__all(ok4(c1)); ++spins) {
          asm volatile("buffer_load_dwordx4 %0, %1, %2, 0 offen sc1\n\ts_waitcnt vmcnt(0)" : "+v"(c1) : "v"(so), "s"(c.rs) : "memory");
          if (spins > SPIN_CAP) { cw_set(c, CW_TMO, 1u); alive = false; break; }
        }
        sc = mfma(sel[0], c1, f32x4{0, 0, 0, 0}); sd = mfma(sel[1], c1, f32x4{0, 0, 0, 0});
      }
      {
        unsigned tv, ts;
        asm volatile("s_cmp_eq_u32 %[go], 0\n\ts_cbranch_scc1 3f\n"
                     "2:\n\tbuffer_load_dwordx4 %[a0], %[off], %[rs], 0 offen sc1\n\tbuffer_load_dwordx4 %[a1], %[off], %[rs], 0 offen offset:1024 sc1\n"
                     "1:\n\t.subsection 1\n"
                     "3:\n\ts_cmp_eq_u32 %[pend], 0\n\ts_cbranch_scc1 1b\n\t"
                     "ds_read_b32 %[tv], %[sa]\n\ts_waitcnt lgkmcnt(0)\n\tv_readfirstlane_b32 %[ts], %[tv]\n\t"
                     "s_cmp_lt_u32 %[ts], %[need]\n\ts_cbranch_scc1 1b\n\t"
                     "s_mov_b32 %[go], 1\n\ts_mov_b32 %[pend], 0\n\ts_mov_b32 %[vp], %[ts]\n\ts_branch 2b\n\t.subsection 0"
                     : [a0] "+v"(vx[0]), [a1] "+v"(vx[1]), [go] "+s"(go), [pend] "+s"(pend), [vp] "+s"(vprog), [tv] "=&v"(tv), [ts] "=&s"(ts)
                     : [off] "v"(vo + (s + 1) * (28 * 16 * 1024)), [rs] "s"(c.rs), [sa] "v"((unsigned)(uintptr_t)((LAS unsigned char*)lds + L_SEEN)), [need] "s"(s + 2) : "memory", "scc");
      }
      const float mb = vmax3(vmax3(sc[0], sc[1], sc[2]), vmax3(sc[3], sd[0], sd[1]), vmax3(sd[2], sd[3], mx));
      const float e0 = ex2(sc[0] - mb), e1 = ex2(sc[1] - mb);
      const float cx = ex2(mx - mb);
      const unsigned pm = pk2(e0, e1);
      const v2u pb = {c16 < 8 ? pm : 0u, c16 < 8 ? 0u : pm};
      f32x4 o = mfma16(zb, pb, ox * cx);
      const f32x4 ls = mfma16(v2u{0x3F803F80u, 0x3F803F80u}, pb, f32x4{0, 0, 0, 0});
      const float rl = __builtin_amdgcn_rcpf(fmaf(cx, lx, ls[0]));
      {
        f32x4 t = o * rl;
        asm volatile("" : "+v"(t));
#pragma unroll
        for (int r = 0; r < 4; ++r) o[r] = add1(t[r], ror8(t[r]));
      }
      if (c16 < 8) *(f32x4*)(lds + L_C + ((w * 8 + c16) * 16 + 4 * gq) * 4) = o;
      const int i = (lane >> 2) & 7, mp = (lane >> 5) * 4 + (lane & 3);
      const int pos = (oc >> 2) * 1024 + (16 * (oc & 3) + 2 * i + (mp & 1)) * 16 + (mp >> 1) * 4;
      int a0 = xch_q((s + 1) & 3, 0, c.cls ^ FIRSTX) + pos, a1 = xch_q((s + 1) & 3, 0, c.cls ^ FIRSTX ^ 2) + pos;
      asm volatile("" : "+v"(a0), "+v"(a1));
      lbar();
      if (__builtin_expect(w == 7, 1)) {
        f32x2 t[8];
#pragma unroll
        for (int k = 0; k < 8; ++k) t[k] = *(const f32x2*)(lds + L_C + ((k * 8 + i) * 16 + 2 * mp) * 4);
        f32x2 sum = ((t[0] + t[1]) + (t[2] + t[3])) + ((t[4] + t[5]) + (t[6] + t[7]));
        const unsigned mw = canon(pk2(sum[0], sum[1]));
        st32s(mw, c.rs, a0);
        st32s(mw, c.rs, a1);
        st32s(SENT, c.rs, xch_q((s + 3) & 3, 0, c.cls) + pos);
        st32s(SENT, c.rs, xch_q((s + 3) & 3, 0, c.cls ^ 2) + pos);
        if (s > 0 && lane == 0) cw_set(c, CW_PM + (64 + oc) * 16, (unsigned)s);
      }
      asm volatile("s_cmp_eq_u32 %1, 0\n\ts_cbranch_scc1 1f\n\ts_waitcnt vmcnt(2)\n\ts_branch 2f\n1:\n\ts_waitcnt vmcnt(0)\n2:" : "+v"(cb) : "s"(go) : "memory", "scc");
    }
    if (!alive) { cw_set(c, CW_TMO, 1u); break; }
  }
  st64s(zb, c.rs, (int)(VMF_OFF + ((size_t)(((NSEG - 1) * 28 + oc) * 8 + w) * 64 + lane) * 8));
  vdrain();
  lbar();
  if (w == 7 && alive && lane == 0) cw_set(c, CW_PM + (64 + oc) * 16, (unsigned)NSEG);
}

#define KV_EARLY 4
#define KV_WAHEAD 3
template <bool V> struct BoolC { static constexpr bool value = V; };
#define KV_NCL 4
constexpr int NCL = KV_NCL;
constexpr int EARLY = KV_EARLY;
constexpr int kv_count(int i) { return i < NCL ? 2 * i + 2 : i + NCL + 1; }
constexpr int XB = 28 * 1024;
DI int xchunk_addr(int q) {
  const int G = q >> 3, tok = G / 7, ks = 2 * (G - 7 * tok) + ((q >> 2) & 1), gq = q & 3;
  return (2 * ks + (tok >> 4)) * 1024 + (gq * 16 + ((tok & 15) ^ (q & 7))) * 16;
}
DI int xslot(int c16, int gq) { return (gq * 16 + (c16 ^ gq)) * 16; }
DI void role_KV(const Params& p, Ctx& c, unsigned char* lds, int hm2, bool helper) {
  const int tid = threadIdx.x, lane = tid & 63, w = __builtin_amdgcn_readfirstlane(tid >> 6);
  const int gq = lane >> 4, c16 = lane & 15;
  const int m = hm2 >> 1, half = hm2 & 1;
  const bool iskk = m < 16;
  const int h = m & 15;
  constexpr int SPB = 28 * 1024, L_SP = 2 * XB;
  const int nit = helper ? NCL : NSEG - NCL, word = CW_PROG + hm2 * 16 + (helper ? 8 : 0);
  auto seg = [&](int i) { return helper ? 2 * i + 1 : (i < NCL ? 2 * i : i + NCL); };
  auto reduce = [&](int i) {
    const int s = seg(i);
    const unsigned char* sp = lds + L_SP + (i & 1) * SPB + lane * 16;
#pragma unroll
    for (int qt = 0; qt < 2; ++qt) {
      f32x4 a0 = *(const f32x4*)(sp + qt * 1024), a1 = *(const f32x4*)(sp + (2 + qt) * 1024);
#pragma unroll
      for (int w2 = 1; w2 < 7; ++w2) { a0 += *(const f32x4*)(sp + (w2 * 4 + qt) * 1024); a1 += *(const f32x4*)(sp + (w2 * 4 + 2 + qt) * 1024); }
      st128s(v4u{pk2(a0[0], a0[1]), pk2(a0[2], a0[3]), pk2(a1[0], a1[1]), pk2(a1[2], a1[3])}, c.rs, (int)(SXX_OFF + ((size_t)(((s * 16 + h) * 2 + half) * 2 + qt) * 64 + lane) * 16));
    }
  };
  if (w == 7) {
    if (helper && lane == 0) cw_set(c, word, 1u);
    const u16* xs = p.x + lane * 8;
    v4u t[28];
#pragma unroll
    for (int f = 0; f < 28; ++f) t[f] = *(const v4u*)(xs + (size_t)seg(0) * (32 * D) + f * 512);
#pragma unroll
    for (int f = 0; f < 28; ++f) *(v4u*)(lds + xchunk_addr(f * 64 + lane)) = t[f];
#pragma unroll
    for (int f = 0; f < 28; ++f) t[f] = *(const v4u*)(xs + (size_t)seg(1) * (32 * D) + f * 512);
    lbar();
    for (int i = 0; i < nit; ++i) {
#pragma unroll
      for (int f = 0; f < 28; ++f) *(v4u*)(lds + ((i + 1) & 1) * XB + xchunk_addr(f * 64 + lane)) = t[f];
      const int sn = seg(i + 2 < nit ? i + 2 : nit - 1);
      MEMBAR();
#pragma unroll
      for (int f = 0; f < 28; ++f) t[f] = *(const v4u*)(xs + (size_t)sn * (32 * D) + f * 512);
      lbar();
      if (lane == 0) {
        if (helper) cw_set(c, word, (unsigned)(2 * i + 3));
        else if (i != EARLY) cw_set(c, word, (unsigned)(i < EARLY ? kv_count(i) : kv_count(i - 1)));
      }
    }
    vdrain();
    lbar();
    if (lane == 0 && !helper) cw_set(c, word, (unsigned)NSEG);
    if (iskk) { reduce(nit - 1); vdrain(); }
    if (lane == 0 && (iskk || helper)) cw_set(c, word, helper ? 255u : (unsigned)(NSEG + 2));
    lbar();
    return;
  }
  wait_vec(c, CW_PF + (m * 8 + 4 * half + (lane & 3)) * 16, 1u, 1);
  constexpr int WAHEAD = KV_WAHEAD;
  v4u wt[2][14];
  auto ldw = [&](int g) {
#pragma unroll
    for (int q = 0; q < 4; ++q) wt[q & 1][2 * g + (q >> 1)] = ld_wf(c, m * 28 + 14 * half + 2 * w + (q & 1), 2 * g + (q >> 1), lane);
    MEMBAR();
  };
#pragma unroll
  for (int g = 0; g < WAHEAD; ++g) ldw(g);
  lbar();
  const int gq2 = (gq >> 1) + 2 * (gq & 1);
  auto segment = [&](auto first, int i) {
    const int s = seg(i);
    const unsigned char* xb = lds + (i & 1) * XB;
    const unsigned char* xf0 = xb + xslot(c16, gq), * xf1 = xb + (xslot(c16, gq) ^ 64);
    f32x4 acc[2][2];
#pragma unroll
    for (int a = 0; a < 2; ++a) { acc[a][0] = f32x4{0, 0, 0, 0}; acc[a][1] = f32x4{0, 0, 0, 0}; }
    v4u xr[3][4];
    auto rdx = [&](int g) {
#pragma unroll
      for (int q = 0; q < 4; ++q) xr[g % 3][q] = *(const v4u*)((q < 2 ? xf0 : xf1) + (4 * g + q) * 1024);
    };
    rdx(0); rdx(1);
    v4u ov[2], xq[2];
    if (iskk) {
#pragma unroll
      for (int qt = 0; qt < 2; ++qt) xq[qt] = *(const v4u*)(xb + ((7 * half + w) * 2 + qt) * 1024 + (xslot(c16, gq2) ^ (((7 * half + w) & 1) << 6)));
#pragma unroll
      for (int g = 0; g < 7; ++g) {
        __builtin_amdgcn_sched_barrier(0);
        if constexpr (decltype(first)::value) { if (g + WAHEAD < 7) ldw(g + WAHEAD); }
        if (g + 2 < 7) rdx(g + 2);
        __builtin_amdgcn_sched_barrier(0);
#pragma unroll
        for (int q = 0; q < 4; ++q) { const int ks = 2 * g + (q >> 1), rt = q & 1; acc[rt][0] = mfma(wt[0][ks], xr[g % 3][q], acc[rt][0]); acc[rt][1] = mfma(wt[1][ks], xr[g % 3][q], acc[rt][1]); }
      }
      __builtin_amdgcn_sched_barrier(0);
#pragma unroll
      for (int rt = 0; rt < 2; ++rt) {
        unsigned x0 = pk2(acc[rt][0][0], acc[rt][0][1]), x1 = pk2(acc[rt][0][2], acc[rt][0][3]);
        unsigned y0 = pk2(acc[rt][1][0], acc[rt][1][1]), y1 = pk2(acc[rt][1][2], acc[rt][1][3]);
        auto s0 = __builtin_amdgcn_permlane16_swap(x0, y0, false, false);
        auto s1 = __builtin_amdgcn_permlane16_swap(x1, y1, false, false);
        ov[rt] = v4u{s0[0], s1[0], s0[1], s1[1]};
      }
    } else {
      xq[0] = xq[1] = v4u{0u, 0u, 0u, 0u};
#pragma unroll
      for (int g = 0; g < 7; ++g) {
        __builtin_amdgcn_sched_barrier(0);
        if constexpr (decltype(first)::value) { if (g + WAHEAD < 7) ldw(g + WAHEAD); }
        if (g + 2 < 7) rdx(g + 2);
        __builtin_amdgcn_sched_barrier(0);
#pragma unroll
        for (int q = 0; q < 4; ++q) { const int ks = 2 * g + (q >> 1), rt = q & 1; acc[rt][0] = mfma(xr[g % 3][q], wt[0][ks], acc[rt][0]); acc[rt][1] = mfma(xr[g % 3][q], wt[1][ks], acc[rt][1]); }
      }
      __builtin_amdgcn_sched_barrier(0);
#pragma unroll
      for (int t2 = 0; t2 < 2; ++t2) ov[t2] = v4u{pk2(acc[0][t2][0], acc[0][t2][1]), pk2(acc[0][t2][2], acc[0][t2][3]), pk2(acc[1][t2][0], acc[1][t2][1]), pk2(acc[1][t2][2], acc[1][t2][3])};
    }
    vdrain();
    if (iskk) {
#pragma unroll
      for (int rt = 0; rt < 2; ++rt) {
        const int g = c16 >> 2, rowq = (c16 & 3) + 4 * rt;
        st128s(ov[rt], c.rs, (int)(KKF_OFF + ((size_t)(((s * 16 + h) * 4 + g) * 14 + 7 * half + w) * 32 + gq2 * 8 + rowq) * 16));
      }
#pragma unroll
      for (int qt = 0; qt < 2; ++qt)
#pragma unroll
        for (int kt = 0; kt < 2; ++kt)
          *(f32x4*)(lds + L_SP + (i & 1) * SPB + (w * 4 + kt * 2 + qt) * 1024 + lane * 16) = mfma(ov[kt], xq[qt], f32x4{0, 0, 0, 0});
      if (w == 3 && i > 0) reduce(i - 1);
    } else {
#pragma unroll
      for (int t2 = 0; t2 < 2; ++t2) st128s(ov[t2], c.rs, (int)(VVF_OFF + ((size_t)((s * 28 + 14 * half + 2 * w + t2) * 16 + h) * 64 + lane) * 16));
    }
    if (helper || i < EARLY) vdrain();
    lbar();
  };
  segment(BoolC<true>{}, 0);
#pragma nounroll
  for (int i = 1; i < nit; ++i) segment(BoolC<false>{}, i);
  vdrain();
  lbar();
  lbar();
}

constexpr int LT_X = 0, LT_PX = 30 * 1024, LT_PM = 62 * 1024;
DI void task_T(const Params& p, const Ctx& c, unsigned char* lds, int seg, int cg, int q0, int nq) {
  constexpr int NQ = 2;
  int tid = threadIdx.x;
  asm volatile("" : "+v"(tid));
  const int lane = tid & 63, w = __builtin_amdgcn_readfirstlane(tid >> 6);
  const int gq = lane >> 4, c16 = lane & 15;
  const int oc = 7 * cg + (w < 7 ? w : 0);
#pragma unroll
  for (int i = 0; i < 4; ++i) {
    const int q = tid + 512 * i;
    if (q < 1792) *(v4u*)(lds + LT_X + xchunk_addr(q)) = *(const v4u*)(p.x + (size_t)seg * (32 * D) + q * 8);
  }
  if (w == 7) {
    wait_vec(c, CW_PROG + lane * 16, lane < 32 ? (unsigned)(seg + 2 > kv_count(EARLY) ? seg + 2 : kv_count(EARLY)) : (unsigned)seg + 1u, 4);
    if (seg < 2 * NCL) wait_vec(c, CW_PROG + lane * 16 + 8, 255u, 4);
  }
  lbar();
  v4u sx[2][2][NQ], vv[16];
#pragma unroll
  for (int hh = 0; hh < 2; ++hh)
#pragma unroll
    for (int hf = 0; hf < 2; ++hf)
#pragma unroll
      for (int qt = 0; qt < NQ; ++qt) sx[hh][hf][qt] = ld128s(c.rs, (int)(SXX_OFF + ((size_t)(((seg * 16 + 2 * w + hh) * 2 + hf) * 2 + ((q0 + qt) & 1)) * 64 + lane) * 16));
  if (w < 7) {
#pragma unroll
    for (int h = 0; h < 16; ++h) vv[h] = ld128s(c.rs, (int)(VVF_OFF + ((size_t)((seg * 28 + oc) * 16 + h) * 64 + lane) * 16));
  }
  float ex[NQ][2][8], mxx[NQ][2], lx[NQ][2];
#pragma unroll
  for (int qt = 0; qt < NQ; ++qt) {
    if (qt >= nq) break;
    const int q = 16 * (q0 + qt) + c16;
#pragma unroll
    for (int hh = 0; hh < 2; ++hh) {
      float m = -1e9f;
#pragma unroll
      for (int k = 0; k < 4; ++k) {
        const unsigned u0 = sx[hh][0][qt][k], u1 = sx[hh][1][qt][k];
        const int key = (k < 2 ? 4 * gq + 2 * k : 16 + 4 * gq + 2 * (k - 2));
        ex[qt][hh][2 * k] = key <= q ? bflo(u0) + bflo(u1) : -1e9f;
        ex[qt][hh][2 * k + 1] = key + 1 <= q ? bfhi(u0) + bfhi(u1) : -1e9f;
        m = vmax(m, vmax(ex[qt][hh][2 * k], ex[qt][hh][2 * k + 1]));
      }
      mxx[qt][hh] = swap32_max(swap16_max(m));
      float l = 0.f;
#pragma unroll
      for (int k = 0; k < 8; ++k) { ex[qt][hh][k] = ex2(ex[qt][hh][k] - mxx[qt][hh]); l += ex[qt][hh][k]; }
      lx[qt][hh] = swap32_add(swap16_add(l));
    }
  }
  if (w == 7) {
    wait_vec(c, CW_PM + (cg * 16 + (seg & 15)) * 16, seg, 4);
    if (seg < NSEG - 2) wait_vec(c, CW_PM + lane * 16, seg + 1, nq == 1 ? 1 : 4);
    else __builtin_amdgcn_s_sleep(TAIL_NAP);
  }
  lbar();
  v4u kf[14]; v2u vm[8];
  auto ld_vm = [&]() {
    MEMBAR();
    wait_vec(c, CW_PM + (64 + oc) * 16, seg + 1, 1);
#pragma unroll
    for (int pr = 0; pr < 8; ++pr) vm[pr] = ld64s(c.rs, (int)(VMF_OFF + ((size_t)((seg * 28 + oc) * 8 + pr) * 64 + lane) * 8));
    MEMBAR();
  };
  f32x4 am[NQ];
  auto vm_ok = [&]() { unsigned mx = 0;
#pragma unroll
    for (int k = 0; k < 8; ++k) mx = umax(mx, umax(vm[k].x, vm[k].y));
    return __all(mx != SENT); };
  auto rd_vm = [&]() {
#pragma unroll
    for (int pr = 0; pr < 8; ++pr) vm[pr] = ld64s(c.rs, (int)(VMF_OFF + ((size_t)((seg * 28 + oc) * 8 + pr) * 64 + lane) * 8));
  };
  const bool direct = seg >= NSEG - 2;
  if (direct) {
    v2u t[2][4];
    for (unsigned spins = 0;; ++spins) {
      unsigned mx = 0;
#pragma unroll
      for (int hh = 0; hh < 2; ++hh)
#pragma unroll
        for (int g = 0; g < 4; ++g) t[hh][g] = ld64s(c.rs, (int)SXM_OFF + (seg - (NSEG - 2)) * 65536 + ((2 * w + hh) * 4 + g) * 1024 + ((16 * q0 + c16) * 8 + 2 * gq) * 4);
      if (seg == NSEG - 1) rd_vm();
#pragma unroll
      for (int k = 0; k < 8; ++k) mx = umax(mx, umax(t[k >> 2][k & 3].x, t[k >> 2][k & 3].y));
      if (__all(mx != SENT)) break;
      if (spins > SPIN_CAP) { cw_set(c, CW_TMO, 1u); break; }
      if ((spins & 255u) == 255u && cw_tmo(c)) break;
      __builtin_amdgcn_s_sleep(1);
      asm volatile("" ::: "memory");
    }
    if (seg != NSEG - 1) {
#pragma unroll
      for (int k = 0; k < 8; ++k) vm[k] = v2u{SENT, SENT};
    }
#pragma unroll
    for (int hh = 0; hh < 2; ++hh)
#pragma unroll
      for (int r = 0; r < 2; ++r)
        am[0][2 * hh + r] = (__uint_as_float(t[hh][0][r]) + __uint_as_float(t[hh][1][r])) + (__uint_as_float(t[hh][2][r]) + __uint_as_float(t[hh][3][r]));
    am[1] = am[0];
  } else {
#pragma unroll
    for (int ks = 0; ks < 14; ++ks) kf[ks] = ld128s(c.rs, (int)(KKM_OFF + (size_t)((seg * 16 + 2 * w + ((c16 >> 1) & 1)) * 56 + 4 * ks + gq) * 128 + (2 * (c16 >> 2) + (c16 & 1)) * 16));
    ld_vm();
#pragma unroll
    for (int qt = 0; qt < NQ; ++qt) {
      const unsigned char* xq = lds + LT_X + ((q0 + qt) & 1) * 1024 + xslot(c16, gq), * xq1 = lds + LT_X + ((q0 + qt) & 1) * 1024 + (xslot(c16, gq) ^ 64);
      f32x4 a = {0, 0, 0, 0}, a2 = {0, 0, 0, 0};
#pragma unroll
      for (int ks = 0; ks < 14; ks += 2) {
        a = mfma(kf[ks], *(const v4u*)(xq + ks * 2048), a);
        a2 = mfma(kf[ks + 1], *(const v4u*)(xq1 + ks * 2048 + 2048), a2);
      }
      am[qt] = a + a2;
    }
  }
#pragma unroll
  for (int qt = 0; qt < NQ; ++qt) {
    if (qt >= nq) break;
    unsigned pm[2];
#pragma unroll
    for (int hh = 0; hh < 2; ++hh) {
      const float mb = vmax(swap32_max(swap16_max(vmax(am[qt][2 * hh], am[qt][2 * hh + 1]))), mxx[qt][hh]);
      const float e0 = ex2(am[qt][2 * hh] - mb), e1 = ex2(am[qt][2 * hh + 1] - mb), cx = ex2(mxx[qt][hh] - mb);
      const float rl = __builtin_amdgcn_rcpf(fmaf(cx, lx[qt][hh], swap32_add(swap16_add(e0 + e1)))), f = cx * rl;
      const float* e = ex[qt][hh];
      *(v4u*)(lds + LT_PX + ((2 * w + hh) * 2 + qt) * 1024 + lane * 16) = v4u{pk2(e[0] * f, e[1] * f), pk2(e[2] * f, e[3] * f), pk2(e[4] * f, e[5] * f), pk2(e[6] * f, e[7] * f)};
      pm[hh] = pk2(e0 * rl, e1 * rl);
    }
    *(v2u*)(lds + LT_PM + (w * 2 + qt) * 512 + lane * 8) = v2u{pm[0], pm[1]};
  }
  if (direct) {
    if (seg != NSEG - 1) __builtin_amdgcn_s_sleep(TAIL_NAP2);
    for (unsigned spins = 0; !vm_ok(); ++spins) {
      asm volatile("" ::: "memory");
      rd_vm();
      if (spins > SPIN_CAP) { cw_set(c, CW_TMO, 1u); break; }
      if ((spins & 255u) == 255u && cw_tmo(c)) break;
      if (spins) __builtin_amdgcn_s_sleep(1);
    }
  }
  lbar();
  if (w < 7) {
#pragma nounroll
    for (int qt = 0; qt < nq; ++qt) {
      f32x4 o = {0, 0, 0, 0}, o2 = {0, 0, 0, 0};
      v4u px[16]; v2u pm[8];
#pragma unroll
      for (int h = 0; h < 16; ++h) px[h] = *(const v4u*)(lds + LT_PX + (h * 2 + qt) * 1024 + lane * 16);
#pragma unroll
      for (int i = 0; i < 8; ++i) pm[i] = *(const v2u*)(lds + LT_PM + (i * 2 + qt) * 512 + lane * 8);
      __builtin_amdgcn_sched_barrier(0);
#pragma unroll
      for (int h = 0; h < 16; h += 2) { o = mfma(vv[h], px[h], o); o2 = mfma(vv[h + 1], px[h + 1], o2); }
#pragma unroll
      for (int i = 0; i < 4; ++i) {
        const v4u a = {vm[2 * i].x, vm[2 * i].y, vm[2 * i + 1].x, vm[2 * i + 1].y}, b = {pm[2 * i].x, pm[2 * i].y, pm[2 * i + 1].x, pm[2 * i + 1].y};
        if (i & 1) o2 = mfma(a, b, o2); else o = mfma(a, b, o);
      }
      o += o2;
      __builtin_amdgcn_raw_buffer_store_b64(v2u{pk2(o[0], o[1]), pk2(o[2], o[3])}, c.ro, ((32 * seg + 16 * (q0 + qt) + c16) * (2 * D) + 16 * oc + 4 * gq) * 2, 0, OUT_AUX);
    }
  }
  lbar();
}

DI unsigned phi2(float a, float b) {
  const unsigned t = pk2(a, b);
  const float x = bflo(t), y = bfhi(t);
  const unsigned u = pk2(x > 0.f ? x + 1.f : __expf(x), y > 0.f ? y + 1.f : __expf(y));
  return pk2(bflo(u) + 1e-6f, bfhi(u) + 1e-6f);
}
constexpr int LB_W = 0, LB_R = 448 * 192;
DI v4u wfrag(const unsigned char* lds, int off) {
  v4s lo = __builtin_amdgcn_ds_read_tr16_b64_v4i16((LAS v4s*)((LAS unsigned char*)lds + off));
  v4s hi = __builtin_amdgcn_ds_read_tr16_b64_v4i16((LAS v4s*)((LAS unsigned char*)lds + off + 4 * 192));
  v2u l2 = __builtin_bit_cast(v2u, lo), h2 = __builtin_bit_cast(v2u, hi);
  return v4u{l2.x, l2.y, h2.x, h2.y};
}
DI void task_B1(const Params& p, const Ctx& c, unsigned char* lds, int h, int tq) {
  const int tid = threadIdx.x, lane = tid & 63, w = __builtin_amdgcn_readfirstlane(tid >> 6);
  const int gq = lane >> 4, c16 = lane & 15;
#pragma unroll 7
  for (int i = 0; i < 21; ++i) {
    const int idx = tid + 512 * i, k = idx / 24, rem = idx - k * 24, m = rem >> 3, sl = rem & 7;
    v2u v = {0u, 0u};
    if (sl < 7) v = *(const v2u*)(p.wqkv + (size_t)k * (3 * D) + m * D + h * 28 + sl * 4);
    *(v2u*)(lds + LB_W + k * 192 + m * 64 + sl * 8) = v;
  }
  if (w < 7) {
    const int nt = 7 * tq + w;
    unsigned e8[8];
#pragma unroll
    for (int i = 0; i < 8; ++i) { const int e = (i < 4 ? 4 * gq + i : 12 + 4 * gq + i); e8[i] = e < 28 ? (unsigned)p.wo[(size_t)(28 * h + e) * D + 16 * nt + c16] : 0u; }
    st128s(v4u{e8[0] | e8[1] << 16, e8[2] | e8[3] << 16, e8[4] | e8[5] << 16, e8[6] | e8[7] << 16}, c.rs, (int)(WOF_OFF + ((size_t)(h * 28 + nt) * 64 + lane) * 16));
  }
  lbar();
  const int t0 = 512 * tq + 64 * w;
  const u16* xr = p.bx + (size_t)(t0 + c16) * D + 8 * gq;
  const int wb = LB_W + (8 * gq + (c16 >> 2)) * 192 + (lane & 3) * 8;
  f32x4 aq[4][2], ak[4][2], av[4][2];
#pragma unroll
  for (int rt = 0; rt < 4; ++rt)
#pragma unroll
    for (int dt = 0; dt < 2; ++dt) { aq[rt][dt] = f32x4{0, 0, 0, 0}; ak[rt][dt] = f32x4{0, 0, 0, 0}; av[rt][dt] = f32x4{0, 0, 0, 0}; }
  v4u xn[4];
#pragma unroll
  for (int rt = 0; rt < 4; ++rt) xn[rt] = *(const v4u*)(xr + (size_t)(16 * rt) * D);
#pragma unroll 1
  for (int ks = 0; ks < 14; ++ks) {
    v4u x[4];
#pragma unroll
    for (int rt = 0; rt < 4; ++rt) x[rt] = xn[rt];
    const int kn = ks + 1 < 14 ? ks + 1 : ks;
#pragma unroll
    for (int rt = 0; rt < 4; ++rt) xn[rt] = *(const v4u*)(xr + (size_t)(16 * rt) * D + 32 * kn);
#pragma unroll
    for (int dt = 0; dt < 2; ++dt) {
      const v4u wq = wfrag(lds, wb + ks * 32 * 192 + dt * 32), wk = wfrag(lds, wb + ks * 32 * 192 + 64 + dt * 32), wv = wfrag(lds, wb + ks * 32 * 192 + 128 + dt * 32);
#pragma unroll
      for (int rt = 0; rt < 4; ++rt) { aq[rt][dt] = mfma(wq, x[rt], aq[rt][dt]); ak[rt][dt] = mfma(x[rt], wk, ak[rt][dt]); av[rt][dt] = mfma(x[rt], wv, av[rt][dt]); }
    }
  }
#pragma unroll
  for (int rt = 0; rt < 4; ++rt)
    st128s(v4u{phi2(aq[rt][0][0], aq[rt][0][1]), phi2(aq[rt][0][2], aq[rt][0][3]), phi2(aq[rt][1][0], aq[rt][1][1]), phi2(aq[rt][1][2], aq[rt][1][3])}, c.rs,
           (int)(PQ_OFF + ((size_t)((t0 / 16 + rt) * 16 + h) * 64 + lane) * 16));
  f32x4 kv[2][2] = {{f32x4{0, 0, 0, 0}, f32x4{0, 0, 0, 0}}, {f32x4{0, 0, 0, 0}, f32x4{0, 0, 0, 0}}};
#pragma unroll
  for (int rp = 0; rp < 2; ++rp) {
    v4u pk[2], vf[2];
#pragma unroll
    for (int dt = 0; dt < 2; ++dt) {
      const f32x4 k0 = ak[2 * rp][dt], k1 = ak[2 * rp + 1][dt], v0 = av[2 * rp][dt], v1 = av[2 * rp + 1][dt];
      pk[dt] = v4u{phi2(k0[0], k0[1]), phi2(k0[2], k0[3]), phi2(k1[0], k1[1]), phi2(k1[2], k1[3])};
      vf[dt] = v4u{pk2(v0[0], v0[1]), pk2(v0[2], v0[3]), pk2(v1[0], v1[1]), pk2(v1[2], v1[3])};
    }
    if (c16 >= 12) pk[1] = v4u{0u, 0u, 0u, 0u};
    if (c16 == 12) vf[1] = v4u{0x3F803F80u, 0x3F803F80u, 0x3F803F80u, 0x3F803F80u};
#pragma unroll
    for (int dt = 0; dt < 2; ++dt)
#pragma unroll
      for (int et = 0; et < 2; ++et) kv[dt][et] = mfma(pk[dt], vf[et], kv[dt][et]);
  }
#pragma unroll
  for (int t = 0; t < 4; ++t) *(f32x4*)(lds + LB_R + (w * 4 + t) * 1024 + lane * 16) = kv[t >> 1][t & 1];
  lbar();
  if (w < 4) {
    f32x4 a = *(const f32x4*)(lds + LB_R + w * 1024 + lane * 16);
#pragma unroll
    for (int w2 = 1; w2 < 8; ++w2) a += *(const f32x4*)(lds + LB_R + (w2 * 4 + w) * 1024 + lane * 16);
    st128s(__builtin_bit_cast(v4u, a), c.rs, (int)(KVP_OFF + ((size_t)((h * 4 + tq) * 4 + w) * 64 + lane) * 16));
  }
  vdrain();
  lbar();
  if (tid == 0) cw_set(c, CW_B1 + h * 4 + tq, 1u);
}

constexpr int LB_OF = 0;
DI void task_B2(const Params& p, const Ctx& c, unsigned char* lds, int tt) {
  const int tid = threadIdx.x, lane = tid & 63, w = __builtin_amdgcn_readfirstlane(tid >> 6);
  const int gq = lane >> 4;
  const int c16 = lane & 15;
  if (w == 0) wait_vec(c, CW_B1 + lane, 1u, 4);
  lbar();
  v4u pq[2][4];
#pragma unroll
  for (int hh = 0; hh < 2; ++hh)
#pragma unroll
    for (int t = 0; t < 4; ++t) pq[hh][t] = ld128s(c.rs, (int)(PQ_OFF + ((size_t)((4 * tt + t) * 16 + 2 * w + hh) * 64 + lane) * 16));
  v4u kva[2][2];
#pragma unroll
  for (int hh = 0; hh < 2; ++hh)
#pragma unroll
    for (int et = 0; et < 2; ++et) {
      f32x4 d0 = {0, 0, 0, 0}, d1 = {0, 0, 0, 0};
#pragma unroll
      for (int tq = 0; tq < 4; ++tq) {
        d0 += __builtin_bit_cast(f32x4, ld128s(c.rs, (int)(KVP_OFF + ((size_t)(((2 * w + hh) * 4 + tq) * 4 + et) * 64 + lane) * 16)));
        d1 += __builtin_bit_cast(f32x4, ld128s(c.rs, (int)(KVP_OFF + ((size_t)(((2 * w + hh) * 4 + tq) * 4 + 2 + et) * 64 + lane) * 16)));
      }
      kva[hh][et] = v4u{pk2(d0[0], d0[1]), pk2(d0[2], d0[3]), pk2(d1[0], d1[1]), pk2(d1[2], d1[3])};
    }
#pragma unroll
  for (int hh = 0; hh < 2; ++hh)
#pragma unroll
    for (int t = 0; t < 4; ++t) {
      const f32x4 n0 = mfma(kva[hh][0], pq[hh][t], f32x4{0, 0, 0, 0}), n1 = mfma(kva[hh][1], pq[hh][t], f32x4{0, 0, 0, 0});
      float den = swap32_add(swap16_add(gq == 3 ? n1[0] : 0.f));
      den = bflo(pk2(bflo(pk2(den, 0.f)) + 1e-6f, 0.f));
      const float rd = 1.f / den;
      const unsigned a0 = pk2(n0[0], n0[1]), a1 = pk2(n0[2], n0[3]), b0 = pk2(n1[0], n1[1]), b1 = pk2(n1[2], n1[3]);
      v4u o = {pk2(bflo(a0) * rd, bfhi(a0) * rd), pk2(bflo(a1) * rd, bfhi(a1) * rd), pk2(bflo(b0) * rd, bfhi(b0) * rd), pk2(bflo(b1) * rd, bfhi(b1) * rd)};
      if (gq == 3) { o.z = 0u; o.w = 0u; }
      *(v4u*)(lds + LB_OF + ((2 * w + hh) * 4 + t) * 1024 + lane * 16) = o;
    }
  lbar();
  f32x4 acc[4][4];
#pragma unroll
  for (int j = 0; j < 4; ++j)
#pragma unroll
    for (int t = 0; t < 4; ++t) acc[j][t] = f32x4{0, 0, 0, 0};
  const int nj = w < 4 ? 4 : 3;
#pragma unroll 2
  for (int h = 0; h < 16; ++h) {
    v4u a[4], b[4];
#pragma unroll
    for (int j = 0; j < 4; ++j) a[j] = ld128s(c.rs, (int)(WOF_OFF + ((size_t)(h * 28 + (j < nj ? w + 8 * j : w)) * 64 + lane) * 16));
#pragma unroll
    for (int t = 0; t < 4; ++t) b[t] = *(const v4u*)(lds + LB_OF + (h * 4 + t) * 1024 + lane * 16);
#pragma unroll
    for (int j = 0; j < 4; ++j)
#pragma unroll
      for (int t = 0; t < 4; ++t) acc[j][t] = mfma(a[j], b[t], acc[j][t]);
  }
#pragma unroll
  for (int j = 0; j < 4; ++j)
    if (j < nj) {
#pragma unroll
      for (int t = 0; t < 4; ++t)
        __builtin_amdgcn_raw_buffer_store_b64(v2u{pk2(acc[j][t][0], acc[j][t][1]), pk2(acc[j][t][2], acc[j][t][3])}, c.ro, ((64 * tt + 16 * t + c16) * (2 * D) + D + 16 * (w + 8 * j) + 4 * gq) * 2, 0, OUT_AUX);
    }
  lbar();
}

DI void role_W(const Params& p, const Ctx& c, unsigned char* lds, int wid) {
  task_B1(p, c, lds, wid >> 2, wid & 3);
  if ((wid & 1) == 0) task_B2(p, c, lds, wid >> 1);
#pragma unroll 1
  for (int k = 0; k < 5; ++k) {
    const int f = wid >= 48 ? wid - 48 : wid + 16;
    const bool half = k == 4;
    if (half ? (wid >= 48 || wid < 16) : (k < 3 || wid < 48))
      task_T(p, c, lds, half ? 60 + (f >> 3) : 16 * k + (wid >> 2), half ? (f >> 1) & 3 : wid & 3, half ? f & 1 : 0, half ? 1 : 2);
  }
}

__global__ void __launch_bounds__(512, 2) k_main(Params p) {
  extern __shared__ __attribute__((aligned(16))) unsigned char lds[];
  const int b = blockIdx.x, raw = b & 7, rank = b >> 3;
  const int tid = threadIdx.x, lane = tid & 63, w = __builtin_amdgcn_readfirstlane(tid >> 6);
  asm volatile("" :: "s"(p.Wq), "s"(p.Wk), "s"(p.Wv), "s"(p.Wo), "s"(p.ws));
  Ctx c;
  c.rs = __builtin_amdgcn_make_buffer_rsrc(p.ws, 0, (int)WS_USED, 0x00020000);
  c.ro = __builtin_amdgcn_make_buffer_rsrc(p.out, 0, 2048 * 2 * D * 2, 0x00020000);
  c.cw = (GAS u64*)(p.ws + CTRL_OFF);
  c.cls = 0;
  u64 old = 0;
  asm volatile("s_cmp_lg_u32 %2, 7\n\ts_cbranch_scc1 1f\n\tglobal_load_dwordx2 %0, %1, off sc1\n1:" : "+v"(old) : "v"(c.cw + CW_EPOCH), "s"(w) : "memory", "scc");
  unsigned pfx = 0;
  {
    u64 pc; asm volatile("s_getpc_b64 %0" : "=s"(pc));
    const GAS unsigned char* cb = (const GAS unsigned char*)(pc & ~127ull);
    const int piece = rank * 8 + w;
    const int piece2 = piece * 384 + lane * 128;
    if (lane < 3 && piece2 < 69101) pfx = *(const GAS unsigned*)(cb + piece2);
  }
  const bool docal = rank == 31;
  unsigned best[4] = {1u << 30, 1u << 30, 1u << 30, 1u << 30};
  int rho;
  {
    const int m = raw * 4 + (rank >> 3), blk = rank & 7;
    if (m < 16) prep_block<true>(p, c, lds, m, blk >> 1, blk & 1, docal, raw * 256, b == 0, old, best);
    else prep_block<false>(p, c, lds, m, blk >> 1, blk & 1, docal, raw * 256, b == 0, old, best);
    asm volatile("" :: "v"(pfx));
    vdrain();
    lbar();
    { const unsigned lo = __builtin_amdgcn_readfirstlane(*(const int*)(lds + 120 * 1024)), hi = __builtin_amdgcn_readfirstlane(*(const int*)(lds + 120 * 1024 + 4));
      c.tagw = ((u64)hi << 32) | lo;
      rho = __builtin_amdgcn_readfirstlane(*(const int*)(lds + 120 * 1024 + 8)); }
    if (tid == 0) cw_set(c, CW_PF + (m * 8 + blk) * 16, 1u);
  }
  const int label = (raw + rho) & 7;
  c.mx = (label & 1) ? MX_ODD : MX_EVEN;
  const bool isS = label < 2, isO = (label == 2 || label == 3) && rank < 14;
  const bool isX = (label == 2 || label == 3) && rank >= 14 && rank < 22;
  const int sid = rank * 2 + label, oid = rank * 2 + (label - 2);
  if (label == 3 && rank == 31 && w == 7) {
    for (unsigned spins = 0; spins < SPIN_CAP; ++spins) {
      const u64 v0 = __hip_atomic_load(c.cw + CW_PF + lane * 16, RLX_AGENT), v1 = __hip_atomic_load(c.cw + CW_PF + (lane + 64) * 16, RLX_AGENT);
      const u64 v2 = __hip_atomic_load(c.cw + CW_PF + (lane + 128) * 16, RLX_AGENT), v3 = __hip_atomic_load(c.cw + CW_PF + (lane + 192) * 16, RLX_AGENT);
      if (__all(cw_ge(c, v0, 1u) && cw_ge(c, v1, 1u) && cw_ge(c, v2, 1u) && cw_ge(c, v3, 1u))) break;
      __builtin_amdgcn_s_sleep(2);
    }
    const unsigned m01 = best[1] < best[0] ? best[1] : best[0], m23 = best[3] < best[2] ? best[3] : best[2];
    const unsigned q = m23 < m01 ? (best[3] < best[2] ? 3u : 2u) : (best[1] < best[0] ? 1u : 0u);
    if (lane == 0) cw_set(c, CW_CLASS, 1u | q << 1);
  }
  if (isS) role_S(p, c, lds, sid >> 2, sid & 3);
  else if (isO) role_O(p, c, lds, oid);
  else if (isX) role_X(p, c, lds, oid - 28);
  else if (label < 4) {}
  else {
    role_KV(p, c, lds, (rank & 15) * 4 + (label - 4), rank >= 16);
    if (rank >= 16) role_W(p, c, lds, (rank - 16) * 4 + (label - 4));
  }
}

static unsigned char* align_ws(void* d_ws) { return (unsigned char*)(((uintptr_t)d_ws + (2u << 20) - 1) & ~(uintptr_t)((2u << 20) - 1)); }
static void launch_v75(void* const* d_in, void* d_out, void* d_ws, hipStream_t stream) {
  Params p;
  p.x = (const u16*)d_in[0]; p.mem0 = (const u16*)d_in[1]; p.Wq = (const u16*)d_in[2]; p.Wk = (const u16*)d_in[3]; p.Wv = (const u16*)d_in[4]; p.Wo = (const u16*)d_in[5];
  p.bx = (const u16*)d_in[7]; p.wqkv = (const u16*)d_in[8]; p.wo = (const u16*)d_in[9];
  p.out = (u16*)d_out; p.ws = align_ws(d_ws);
  (void)hipFuncSetAttribute((const void*)k_main, hipFuncAttributeMaxDynamicSharedMemorySize, 128 * 1024);
  k_main<<<256, 512, 128 * 1024, stream>>>(p);
}
extern "C" void kernel_launch(void* const* d_in, const int* in_sizes, int n_in, void* d_out, int out_size, void* d_ws, size_t ws_size,
                              hipStream_t stream) {
  (void)in_sizes; (void)n_in; (void)out_size; (void)ws_size;
  launch_v75(d_in, d_out, d_ws, stream);
}
```
